```python
import torch

def _softmax(x, axis=-1):
    return torch.softmax(x, dim=axis)
def _gelu(x):
    return torch.nn.functional.gelu(x, approximate="tanh")
def _silu(x):
    return torch.nn.functional.silu(x)
def _sigmoid(x):
    return torch.sigmoid(x)
def _layernorm(x, axis=-1, eps=1e-5):
    m = x.mean(dim=axis, keepdim=True)
    v = x.var(dim=axis, keepdim=True, unbiased=False)
    return (x - m) / torch.sqrt(v + eps)
def _scatter_add(tgt, idx, src, axis=0):
    return tgt.clone().index_add_(axis, idx, src)
def _segment_sum(src, seg, n):
    out = torch.zeros((n,) + src.shape[1:], dtype=src.dtype)
    return out.index_add_(0, seg, src)
def _pad(x, pw):
    flat = tuple(v for pair in reversed(pw) for v in pair)
    return torch.nn.functional.pad(x, flat)

kernel_name = 'sequential_preLN__compressed_kv_cla__dense_activation_gelu__prompt'

def setup_inputs():

    _w = (lambda a: (lambda t: t.to(torch.bfloat16) if t.is_floating_point() else t)(torch.as_tensor(a)))
    def _A_setup():
        B, H, Sq, Sk, D, M = 1, 32, 2048, 2048, 128, 512
        Dc = 4 * D
        return {
            "x": _w((lambda *s: torch.randn(*s, dtype=torch.bfloat16) * 0.05)(B, Sq, M)),
            "c_kv": _w((lambda *s: torch.randn(*s, dtype=torch.bfloat16) * 0.2)(B, Sk, Dc)),
            "w_q": _w((lambda *s: torch.randn(*s, dtype=torch.bfloat16) * 6.4)(M, H, D) * (M ** -0.5)),
            "w_uk": _w((lambda *s: torch.randn(*s, dtype=torch.bfloat16) * 12.8)(Dc, H, D) * (Dc ** -0.5)),
            "w_uv": _w((lambda *s: torch.randn(*s, dtype=torch.bfloat16) * 0.2)(Dc, H, D) * (Dc ** -0.5)),
            "w_o": _w((lambda *s: torch.randn(*s, dtype=torch.bfloat16) * 0.2)(H, D, M) * ((H * D) ** -0.5)),
        }
    _A_in = _A_setup()

    def _B_setup():
        B, S, D, F = 1, 2048, 512, 1376
        return {
            "x": _w((lambda *s: torch.randn(*s, dtype=torch.bfloat16) * 0.05)(B, S, D)),
            "w1": _w((lambda *s: torch.randn(*s, dtype=torch.bfloat16) * 0.4)(D, F) * (D ** -0.5)),
            "b1": _w((lambda *s: torch.randn(*s, dtype=torch.bfloat16) * 12.8)(F) * 0.01),
            "w2": _w((lambda *s: torch.randn(*s, dtype=torch.bfloat16) * 0.2)(F, D) * (F ** -0.5)),
            "b2": _w((lambda *s: torch.randn(*s, dtype=torch.bfloat16) * 1.6)(D) * 0.01),
        }
    _B_in = _B_setup()

    return {
        "A_x": _A_in["x"],
        "A_c_kv": _A_in["c_kv"],
        "A_w_q": _A_in["w_q"],
        "A_w_uk": _A_in["w_uk"],
        "A_w_uv": _A_in["w_uv"],
        "A_w_o": _A_in["w_o"],
        "B_x": _B_in["x"],
        "B_w1": _B_in["w1"],
        "B_b1": _B_in["b1"],
        "B_w2": _B_in["w2"],
        "B_b2": _B_in["b2"],
    }

def reference(A_x, A_c_kv, A_w_q, A_w_uk, A_w_uv, A_w_o, B_x, B_w1, B_b1, B_w2, B_b2):
    def _A_ref(x, c_kv, w_q, w_uk, w_uv, w_o):
        B, Sq, M = x.shape[0], x.shape[1], x.shape[2]
        Sk = c_kv.shape[1]
        H, D = w_q.shape[1], w_q.shape[2]
        q = torch.einsum("bqm,mhd->bhqd", x, w_q)
        k = torch.einsum("bkc,chd->bhkd", c_kv, w_uk)
        v = torch.einsum("bkc,chd->bhkd", c_kv, w_uv)
        scores = torch.einsum("bhqd,bhkd->bhqk", q, k) * (D ** -0.5)
        off = Sk - Sq
        qi = torch.arange(Sq)[:, None]
        kj = torch.arange(Sk)[None, :]
        cmask = kj <= (qi + off)
        scores = torch.where(cmask[None, None], scores, -1e9)
        p = _softmax(scores, axis=-1)
        ctx = torch.einsum("bhqk,bhkd->bhqd", p, v)
        return torch.einsum("bhqd,hdm->bqm", ctx, w_o)

    def _B_ref(x, w1, b1, w2, b2):
        h = torch.einsum("bsd,df->bsf", x, w1) + b1
        h = _gelu(h)
        return torch.einsum("bsf,fd->bsd", h, w2) + b2

    x = A_x
    h = x + _A_ref(_layernorm(x, axis=-1), A_c_kv, A_w_q, A_w_uk, A_w_uv, A_w_o)
    h2 = h + _B_ref(_layernorm(h, axis=-1), B_w1, B_b1, B_w2, B_b2)
    return h2
```

```cpp
#define AT_PRIO 0
#define F1P 3
#define ALN 1
#define GS 1408
#define WQ_T 9
#define SU0 0
#define SU1 0
#define SU2 0
#define SCH0 0x11111111u
#define SCH1 0x11111111u
#define SCH2 0u
#define SCH3 0u
#define PUBC 3
#define PUBS 0
#define XN_T 1
#define WARM_S 2
#define EARLYP 1
#define WARM_H 0
#include <hip/hip_runtime.h>
#include <hip/hip_bf16.h>
#include <cstdio>
#include <cstdint>

enum { ORDER_NATURAL = 0, ORDER_REVERSED = 1, ORDER_PAIRED = 2, ORDER_XCD = 4 };
using TIN  = __hip_bfloat16;
using TOUT = __hip_bfloat16;
constexpr int B = 1, H = 32, HKV = 32, SQ = 2048, SKV = 2048, D = 128;
constexpr int QOFF = 0;
constexpr int WINDOW = SKV;
constexpr float THR = 0.f;
constexpr int ORDER = ORDER_PAIRED;
constexpr int GRID_WGS = 0;
constexpr bool WSKIP = WINDOW <= 512;
constexpr float SCALE = 0.08838834764831845f;
constexpr int NW = 8, QBLK = 32, KVBLK = 64, QB = NW * QBLK;
constexpr int SHM_V = KVBLK * D * 2, SHM_K = KVBLK * D * 2;
constexpr int LDS_BYTES = 2 * SHM_V + 2 * SHM_K + NW * 64 * 4;
static_assert(D == 128 && SQ % QB == 0 && SKV % KVBLK == 0 && H % HKV == 0 && QOFF >= 0 && QOFF + SQ <= SKV && WINDOW >= 1, "geometry");

using bf16 = __hip_bfloat16;
typedef short bf16x8 __attribute__((ext_vector_type(8)));
typedef short s16x4 __attribute__((ext_vector_type(4)));
typedef float f32x16 __attribute__((ext_vector_type(16)));
typedef float f32x4 __attribute__((ext_vector_type(4)));
typedef unsigned u32x4 __attribute__((ext_vector_type(4)));
template <class A, class Bt> struct same_t { static constexpr bool v = false; };
template <class A> struct same_t<A, A> { static constexpr bool v = true; };

#define KSWZ(row, colB) ((row) * 256 + ((colB) ^ (((row) & 7) << 4)))
#define SBAR() __builtin_amdgcn_sched_barrier(0)
__device__ __forceinline__ int v_st(int k, int c) { const int kk = (k & ~0xC) | ((k & 4) << 1) | ((k & 8) >> 1); return ((kk >> 3) * 4 + (c >> 5)) * 512 + ((kk & 7) * 32 + (c & 31)) * 2; }
__device__ __forceinline__ int v_rd_base(int lane) { return ((lane & 3) << 3) | (((lane >> 2) & 3) << 6) | (((lane >> 4) & 1) << 5) | (((lane >> 5) & 1) << 8); }
constexpr int v_rd_off(int d0, int ks, int half) { return d0 * 512 + ks * 4096 + half * 2048; }
__device__ __forceinline__ int crow(int r, int hi) { return (r & 3) + 8 * (r >> 2) + 4 * hi; }
__device__ __forceinline__ unsigned cvtpk(float lo, float hi) {
    unsigned r; asm volatile("v_cvt_pk_bf16_f32 %0, %1, %2" : "=v"(r) : "v"(lo), "v"(hi)); return r;
}
__device__ __forceinline__ bf16x8 pack8(f32x4 a, f32x4 b) {
    u32x4 w = {cvtpk(a[0], a[1]), cvtpk(a[2], a[3]), cvtpk(b[0], b[1]), cvtpk(b[2], b[3])};
    return *reinterpret_cast<bf16x8*>(&w);
}
template <class T> __device__ __forceinline__ bf16x8 load8(const T* p) {
    if constexpr (same_t<T, float>::v) { return pack8(*(const f32x4*)p, *(const f32x4*)(p + 4)); }
    else { return *reinterpret_cast<const bf16x8*>(p); }
}
__device__ __forceinline__ void mask_tile(f32x16& p0, f32x16& p1, int dq, unsigned W) {
    const float NEG = -__builtin_inff();
#pragma unroll
    for (int r = 0; r < 16; ++r) {
        const int c = (r & 3) + 8 * (r >> 2);
        if ((unsigned)(dq - c) >= W) p0[r] = NEG;
        if ((unsigned)(dq - c - 32) >= W) p1[r] = NEG;
    }
}
__device__ __forceinline__ void partialSM(f32x16& p0, f32x16& p1, float& m_reg, float& mn, float& alpha) {
    float pmax = p0[0]; for (int r = 1; r < 16; ++r) pmax = fmaxf(pmax, p0[r]); for (int r = 0; r < 16; ++r) pmax = fmaxf(pmax, p1[r]);
    { auto rr = __builtin_amdgcn_permlane32_swap(__float_as_uint(pmax), __float_as_uint(pmax), false, false);
      pmax = fmaxf(__uint_as_float(rr[0]), __uint_as_float(rr[1])); }
    constexpr float C2 = 1.4426950408889634f * SCALE;
    if (__builtin_expect(__all((pmax - m_reg) * SCALE <= THR), 1)) { mn = m_reg; alpha = 1.f; }
    else { mn = fmaxf(m_reg, pmax); alpha = __builtin_amdgcn_exp2f((m_reg - mn) * C2); m_reg = mn; }
    const float mnL = -mn * C2;
    for (int r = 0; r < 16; ++r) p0[r] = fmaf(p0[r], C2, mnL); for (int r = 0; r < 16; ++r) p1[r] = fmaf(p1[r], C2, mnL);
    for (int r = 0; r < 16; ++r) p0[r] = __builtin_amdgcn_exp2f(p0[r]);
}
__device__ __forceinline__ void finishSM(f32x16& p0, f32x16& p1, float alpha, float& l_reg, bf16x8& pa0, bf16x8& pa1, bf16x8& pa2, bf16x8& pa3) {
    for (int r = 0; r < 16; ++r) p1[r] = __builtin_amdgcn_exp2f(p1[r]);
    float ps = 0; for (int r = 0; r < 16; ++r) ps += p0[r]; for (int r = 0; r < 16; ++r) ps += p1[r];
    { auto rr = __builtin_amdgcn_permlane32_swap(__float_as_uint(ps), __float_as_uint(ps), false, false);
      ps = __uint_as_float(rr[0]) + __uint_as_float(rr[1]); }
    l_reg = l_reg * alpha + ps;
#define PK4(P, B_, OUT) do { unsigned a0 = cvtpk(P[B_+0], P[B_+1]), a1 = cvtpk(P[B_+2], P[B_+3]);                          \
        unsigned b0 = cvtpk(P[B_+4], P[B_+5]), b1 = cvtpk(P[B_+6], P[B_+7]);                                             \
        auto r0 = __builtin_amdgcn_permlane32_swap(a0, b0, false, false); auto r1 = __builtin_amdgcn_permlane32_swap(a1, b1, false, false); \
        u32x4 w = {r0[0], r1[0], r0[1], r1[1]}; OUT = *reinterpret_cast<bf16x8*>(&w); } while (0)
    PK4(p0, 0, pa0); PK4(p0, 8, pa1); PK4(p1, 0, pa2); PK4(p1, 8, pa3);
#undef PK4
}
template <int KB, bool SK>
__device__ __forceinline__ void qkt(f32x16& p0, f32x16& p1, const char* K_lds, int r32, int hi, const bf16x8* qr, bool act) {
    if (SK && !act) { const float NEG = -__builtin_inff();
#pragma unroll
        for (int r = 0; r < 16; ++r) { p0[r] = NEG; p1[r] = NEG; } return; }
    p0 = f32x16{}; p1 = f32x16{};
    const char* kb[4];
#pragma unroll
    for (int dd = 0; dd < 4; ++dd) kb[dd] = K_lds + KB * SHM_K + KSWZ(r32, (dd * 16 + hi * 8) * 2);
#pragma unroll
    for (int d0 = 0; d0 < 8; ++d0) { const char* a = kb[d0 & 3] + (d0 >> 2) * 128;
        bf16x8 b0 = *reinterpret_cast<const bf16x8*>(a);
        bf16x8 b1 = *reinterpret_cast<const bf16x8*>(a + 32 * 256);
        p0 = __builtin_amdgcn_mfma_f32_32x32x16_bf16(b0, qr[d0], p0, 0, 0, 0);
        p1 = __builtin_amdgcn_mfma_f32_32x32x16_bf16(b1, qr[d0], p1, 0, 0, 0); }
}
template <int VB, bool SK>
__device__ __forceinline__ void pv_tile(f32x16* o, int vb0, bf16x8 pa0, bf16x8 pa1, bf16x8 pa2, bf16x8 pa3, bool act) {
    if (SK && !act) return;
#define TRRD(dst, off) asm volatile("ds_read_b64_tr_b16 %0, %1 offset:%2" : "=&v"(dst) : "v"(vb0), "i"(off) : "memory")
#define PV_D0(d0) do { s16x4 l0, l1, l2, l3, h0, h1, h2, h3; constexpr int b_ = VB * SHM_V + v_rd_off(d0, 0, 0);     \
        TRRD(l0, b_); TRRD(h0, b_ + 2048); TRRD(l1, b_ + 4096); TRRD(h1, b_ + 6144); TRRD(l2, b_ + 8192); TRRD(h2, b_ + 10240); TRRD(l3, b_ + 12288); TRRD(h3, b_ + 14336); \
        asm volatile("s_waitcnt lgkmcnt(0)" ::: "memory"); SBAR();                 \
        o[d0] = __builtin_amdgcn_mfma_f32_32x32x16_bf16(pa0, (bf16x8){l0[0], l0[1], l0[2], l0[3], h0[0], h0[1], h0[2], h0[3]}, o[d0], 0, 0, 0);   \
        o[d0] = __builtin_amdgcn_mfma_f32_32x32x16_bf16(pa1, (bf16x8){l1[0], l1[1], l1[2], l1[3], h1[0], h1[1], h1[2], h1[3]}, o[d0], 0, 0, 0);   \
        o[d0] = __builtin_amdgcn_mfma_f32_32x32x16_bf16(pa2, (bf16x8){l2[0], l2[1], l2[2], l2[3], h2[0], h2[1], h2[2], h2[3]}, o[d0], 0, 0, 0);   \
        o[d0] = __builtin_amdgcn_mfma_f32_32x32x16_bf16(pa3, (bf16x8){l3[0], l3[1], l3[2], l3[3], h3[0], h3[1], h3[2], h3[3]}, o[d0], 0, 0, 0); } while (0)
    PV_D0(0); PV_D0(1); PV_D0(2); PV_D0(3);
#undef PV_D0
#undef TRRD
}

template <class TIn, class TOut> struct BlockRef { const TIn* Q; const TIn* K; const TIn* V; TOut* O; int P0; };
template <class TIn> struct Seam {
    bf16x8 qr[8];
    bf16x8 st_v0, st_v1, st_k0, st_k1; f32x4 sf0, sf1, sf2, sf3;
    f32x4 tq[16];
};
__device__ __forceinline__ int swa_jlo(int P0, int W) { const int lowk = P0 - W + 1; return lowk > 0 ? lowk / KVBLK : 0; }
#define ROW(p, k0, rr) ((p) + (size_t)((k0) + (rr)) * D + sc)
#define VMW() asm volatile("s_waitcnt vmcnt(0)" ::: "memory")
#define VMWN(n) asm volatile("s_waitcnt vmcnt(%0)" :: "i"(n) : "memory")
#define SLOAD_H(Kp, Vp, k0) do { S.st_v0 = load8<TIn>(ROW(Vp, k0, sr)); S.st_v1 = load8<TIn>(ROW(Vp, k0, 32 + sr));              \
                         S.st_k0 = load8<TIn>(ROW(Kp, k0, sr)); S.st_k1 = load8<TIn>(ROW(Kp, k0, 32 + sr)); } while (0)
#define SWRITE_HK(bf) do { *(bf16x8*)(K_lds + (bf) * SHM_K + kws) = S.st_k0; *(bf16x8*)(K_lds + (bf) * SHM_K + kws + 32 * 256) = S.st_k1; } while (0)
#define SWRITE_HV(bf) do { *(bf16x8*)(V_lds + (bf) * SHM_V + vst0) = S.st_v0; *(bf16x8*)(V_lds + (bf) * SHM_V + vst1) = S.st_v1; } while (0)
#define SWRITE_H(bf) do { SWRITE_HV(bf); SWRITE_HK(bf); } while (0)
#define SLOAD_F(p, k0) do { S.sf0 = *(const f32x4*)ROW(p, k0, sr); S.sf1 = *(const f32x4*)(ROW(p, k0, sr) + 4);                \
                            S.sf2 = *(const f32x4*)ROW(p, k0, 32 + sr); S.sf3 = *(const f32x4*)(ROW(p, k0, 32 + sr) + 4); } while (0)
#define SWRITE_KF(bf) do { *(bf16x8*)(K_lds + (bf) * SHM_K + kws) = pack8(S.sf0, S.sf1); *(bf16x8*)(K_lds + (bf) * SHM_K + kws + 32 * 256) = pack8(S.sf2, S.sf3); } while (0)
#define SWRITE_VF(bf) do { *(bf16x8*)(V_lds + (bf) * SHM_V + vst0) = pack8(S.sf0, S.sf1); *(bf16x8*)(V_lds + (bf) * SHM_V + vst1) = pack8(S.sf2, S.sf3); } while (0)
template <class TIn, class TOut>
__device__ __forceinline__ void causal_swa_prime(const BlockRef<TIn, TOut>& cur, int W, char* lds, Seam<TIn>& S) {
    constexpr bool F32 = same_t<TIn, float>::v;
    const int tid = threadIdx.x, wid = __builtin_amdgcn_readfirstlane(tid >> 6), lane = tid & 63, r32 = lane & 31, hi = lane >> 5;
    const int sr = tid >> 4, sc = (tid & 15) * 8, kws = KSWZ(sr, sc * 2); char* K_lds = lds + 2 * SHM_V;
    const int kb0 = swa_jlo(cur.P0, W) * KVBLK;
    for (int d0 = 0; d0 < 8; ++d0) S.qr[d0] = load8<TIn>(cur.Q + (size_t)(wid * QBLK + r32) * D + d0 * 16 + hi * 8);
    if constexpr (F32) { SLOAD_F((const float*)cur.K, kb0); VMW(); SWRITE_KF(0); SBAR(); SLOAD_F((const float*)cur.V, kb0); }
    else { SLOAD_H(cur.K, cur.V, kb0); VMW(); SWRITE_HK(0); }
    __syncthreads();
}
template <class TIn, class TOut>
__device__ __forceinline__ void causal_swa_block(const BlockRef<TIn, TOut>& cur, const BlockRef<TIn, TOut>& nxt, int skv, int W, char* lds, Seam<TIn>& S) {
    constexpr bool F32 = same_t<TIn, float>::v;
    const int tid = threadIdx.x, wid = __builtin_amdgcn_readfirstlane(tid >> 6), lane = tid & 63, r32 = lane & 31, hi = lane >> 5;
    const int j_lo = swa_jlo(cur.P0, W);
    int j_hi = (cur.P0 + QB - 1) / KVBLK + 1; if (j_hi > skv / KVBLK) j_hi = skv / KVBLK;
    const int NT = j_hi - j_lo;
    const int kbn = swa_jlo(nxt.P0, W) * KVBLK;
    const int qlo = cur.P0 + wid * QBLK, qm = qlo + r32 - 4 * hi;
    char* V_lds = lds; char* K_lds = lds + 2 * SHM_V;
    float* ws = (float*)(lds + 2 * SHM_V + 2 * SHM_K) + wid * 64; float* li_l = ws, * al_l = ws + 32;
    float m_reg = -1e30f, l_reg = 0; f32x16 o[4] = {};
    const int sr = tid >> 4, sc = (tid & 15) * 8, vst0 = v_st(sr, sc), vst1 = v_st(32 + sr, sc), kws = KSWZ(sr, sc * 2);
    const int vb0 = (int)(uintptr_t)V_lds + v_rd_base(lane);
    const TIn* Kh = cur.K; const TIn* Vh = cur.V;
#define RESC(a) do { if (__any((a) < 1.f)) { if (hi == 0) al_l[r32] = (a); asm volatile("s_waitcnt lgkmcnt(0)" ::: "memory");              \
                     for (int d_ = 0; d_ < 4; ++d_) for (int r = 0; r < 16; ++r) o[d_][r] *= al_l[crow(r, hi)]; } } while (0)
#define KBASE(t) ((j_lo + (t)) * KVBLK)
#define ACT(t) (KBASE(t) <= qlo + QBLK - 1 && KBASE(t) + KVBLK - 1 >= qlo - W + 1)
#define MASKT(P0_, P1_, t) do { const int kb_ = KBASE(t); if ((!SK || ACT(t)) && (kb_ + KVBLK - 1 > qlo || kb_ <= qlo + QBLK - 1 - W)) mask_tile(P0_, P1_, qm - kb_, (unsigned)W); } while (0)
    constexpr int NQL = F32 ? 16 : 8;
    constexpr bool SK = WSKIP && !F32;
#define SEAM_K0() do { VMWN(NQL); if constexpr (F32) { SWRITE_KF(0); SBAR(); SLOAD_F((const float*)nxt.V, kbn); } else { SWRITE_HK(0); } SBAR(); } while (0)
    f32x16 pA0, pA1, pB0, pB1; float mnA, mnB, alA, alB; bf16x8 pa0, pa1, pa2, pa3;
    if constexpr (F32) { VMW(); SWRITE_VF(0); SBAR(); } else { SWRITE_HV(0); SBAR(); }
    if (NT > 1) { if constexpr (F32) SLOAD_F((const float*)Kh, KBASE(1)); else SLOAD_H(Kh, Vh, KBASE(1)); }
    SBAR(); qkt<0, SK>(pA0, pA1, K_lds, r32, hi, S.qr, ACT(0));
    if constexpr (F32) { if (NT > 1) { VMW(); SWRITE_KF(1); SBAR(); SLOAD_F((const float*)Vh, KBASE(1)); } }
    MASKT(pA0, pA1, 0); partialSM(pA0, pA1, m_reg, mnA, alA);
    if (NT > 1) { VMW(); if constexpr (F32) { SWRITE_VF(1); SBAR(); if (NT > 2) SLOAD_F((const float*)Kh, KBASE(2)); } else SWRITE_H(1); }
    __syncthreads();
#define HALF_STEP(PX0, PX1, mnX, alX, PY0, PY1, alY, t, KB, VB, SB) do {                                                      \
        SBAR(); qkt<KB, SK>(PX0, PX1, K_lds, r32, hi, S.qr, ACT(t));                                             \
        finishSM(PY0, PY1, alY, l_reg, pa0, pa1, pa2, pa3); SBAR();                                                           \
        if ((t) + 1 < NT) { if constexpr (F32) { VMW(); SWRITE_KF(SB); SBAR(); SLOAD_F((const float*)Vh, KBASE((t) + 1)); }  \
                            else { SLOAD_H(Kh, Vh, KBASE((t) + 1)); } SBAR(); }                                               \
        pv_tile<VB, SK>(o, vb0, pa0, pa1, pa2, pa3, ACT((t) - 1)); MASKT(PX0, PX1, (t)); partialSM(PX0, PX1, m_reg, mnX, alX);                                        \
        __syncthreads();                                                                                                      \
        if ((t) + 1 < NT) { VMW(); if constexpr (F32) { SWRITE_VF(SB); SBAR(); if ((t) + 2 < NT) SLOAD_F((const float*)Kh, KBASE((t) + 2)); } \
                            else { SWRITE_H(SB); } }                                                                          \
        RESC(alX); __syncthreads(); } while (0)
    for (int t = 1; t + 1 < NT; t += 2) {
        HALF_STEP(pB0, pB1, mnB, alB, pA0, pA1, alA, t, 1, 0, 0);
        HALF_STEP(pA0, pA1, mnA, alA, pB0, pB1, alB, t + 1, 0, 1, 1);
    }
    const bool even = (NT & 1) == 0;
    if (even) { SBAR(); qkt<1, SK>(pB0, pB1, K_lds, r32, hi, S.qr, ACT(NT - 1)); SBAR(); }
#define QROW(e) (nxt.Q + (size_t)(wid * QBLK + r32) * D + ((e) >> 1) * 16 + hi * 8 + ((e) & 1) * 4)
    if constexpr (F32) { SLOAD_F((const float*)nxt.K, kbn); SBAR();
#pragma unroll
        for (int e = 0; e < 8; ++e) S.tq[e] = *(const f32x4*)QROW(e); }
    else { SLOAD_H(nxt.K, nxt.V, kbn); SBAR();
#pragma unroll
        for (int d0 = 0; d0 < 8; ++d0) S.qr[d0] = load8<TIn>(nxt.Q + (size_t)(wid * QBLK + r32) * D + d0 * 16 + hi * 8); }
    SBAR();
    finishSM(pA0, pA1, alA, l_reg, pa0, pa1, pa2, pa3); SBAR();
    if constexpr (F32) {
#pragma unroll
        for (int e = 8; e < 16; ++e) S.tq[e] = *(const f32x4*)QROW(e); SBAR(); }
#undef QROW
    pv_tile<0, SK>(o, vb0, pa0, pa1, pa2, pa3, ACT(even ? NT - 2 : NT - 1));
    if (even) { MASKT(pB0, pB1, NT - 1); partialSM(pB0, pB1, m_reg, mnB, alB); __syncthreads(); RESC(alB);
        finishSM(pB0, pB1, alB, l_reg, pa0, pa1, pa2, pa3); SBAR(); pv_tile<1, SK>(o, vb0, pa0, pa1, pa2, pa3, ACT(NT - 1)); }
    SBAR(); SEAM_K0();
    if (hi == 0) li_l[r32] = l_reg; asm volatile("s_waitcnt lgkmcnt(0)" ::: "memory");
    float rli[16];
#pragma unroll
    for (int r = 0; r < 16; ++r) rli[r] = __builtin_amdgcn_rcpf(li_l[crow(r, hi)]);
    TOut* Ow = cur.O + (size_t)(wid * QBLK) * D;
#pragma unroll
    for (int r = 0; r < 16; ++r) { const int orow = crow(r, hi);
#pragma unroll
        for (int d0 = 0; d0 < 4; ++d0) { const float v = o[d0][r] * rli[r];
            if constexpr (same_t<TOut, float>::v) { Ow[(size_t)orow * D + d0 * 32 + r32] = v; }
            else { const float vn = __shfl_xor(v, 1);
                   if ((r32 & 1) == 0) *(unsigned*)(Ow + (size_t)orow * D + d0 * 32 + r32) = cvtpk(v, vn); } } }
    if constexpr (F32) {
#pragma unroll
        for (int d0 = 0; d0 < 8; ++d0) S.qr[d0] = pack8(S.tq[2 * d0], S.tq[2 * d0 + 1]); }
    __syncthreads();
#undef RESC
#undef KBASE
#undef ACT
#undef MASKT
#undef SEAM_K0
#undef HALF_STEP
}
#undef ROW
#undef VMW
#undef VMWN
#undef SLOAD_H
#undef SWRITE_HK
#undef SWRITE_HV
#undef SWRITE_H
#undef SLOAD_F
#undef SWRITE_KF
#undef SWRITE_VF

__host__ __device__ inline int swa_nramp(int nqb, int W, int qoff) { const int t = W - 1 - qoff; const int n = t < 0 ? 0 : t / QB + 1; return n > nqb ? nqb : n; }
__host__ __device__ inline int swa_nx(int nqb, int nramp, int order) { return (order & ORDER_PAIRED) ? (nramp + 1) / 2 + (nqb - nramp) : nqb; }
struct SwaItem { int bh, qb0, qb1; };
__device__ __forceinline__ SwaItem swa_decode(int L, int nb, int nh, int nhkv, int nqb, int nx, int nramp, int order) {
    const int G = nh / nhkv; SwaItem it; int x;
    if ((order & ORDER_XCD) && (nb * nhkv) % 8 == 0) { const int xcd = L & 7, k = L >> 3, per = G * nx, gi = k / per, r = k - gi * per;
        it.bh = (gi * 8 + xcd) * G + r / nx; x = r % nx; }
    else { it.bh = L / nx; x = L - it.bh * nx; }
    if (order & ORDER_PAIRED) { const int ns = nqb - nramp;
        if (x < ns) { it.qb0 = it.qb1 = nqb - 1 - x; } else { it.qb0 = x - ns; it.qb1 = nramp - 1 - it.qb0; } }
    else { it.qb0 = it.qb1 = ((order & 3) == ORDER_REVERSED) ? nqb - 1 - x : x; }
    return it;
}
template <class TIn, class TOut>
__device__ __forceinline__ BlockRef<TIn, TOut> swa_ref(const SwaItem& it, int pass, const TIn* Q, const TIn* K, const TIn* V, TOut* O,
                                                    int nh, int nhkv, int sq, int skv, int qoff) {
    const int qb = pass ? it.qb1 : it.qb0, kvh = it.bh / (nh / nhkv);
    BlockRef<TIn, TOut> r;
    r.Q = Q + ((size_t)it.bh * sq + (size_t)qb * QB) * D; r.O = O + ((size_t)it.bh * sq + (size_t)qb * QB) * D;
    r.K = K + (size_t)kvh * skv * D; r.V = V + (size_t)kvh * skv * D; r.P0 = qoff + qb * QB;
    return r;
}
template <class TIn, class TOut>
__global__ __launch_bounds__(64 * NW, 1)
void attn_fwd_causal_swa(const TIn* __restrict__ Q, const TIn* __restrict__ K, const TIn* __restrict__ V, TOut* __restrict__ O,
                         const int* __restrict__ Wdev, int nb, int nh, int nhkv, int sq, int skv, int W, int qoff, int order, int nramp) {
    extern __shared__ __attribute__((aligned(16))) char lds[];
    if (Wdev) W = *Wdev;
    const int nqb = sq / QB, nx = swa_nx(nqb, nramp, order), total = nx * nb * nh, stride = gridDim.x;
    int L = blockIdx.x; if (L >= total) return;
    SwaItem it = swa_decode(L, nb, nh, nhkv, nqb, nx, nramp, order); int pass = 0;
    BlockRef<TIn, TOut> cur = swa_ref<TIn, TOut>(it, 0, Q, K, V, O, nh, nhkv, sq, skv, qoff);
    Seam<TIn> S;
    causal_swa_prime<TIn, TOut>(cur, W, lds, S);
    for (;;) {
        const bool more_pass = pass == 0 && it.qb1 != it.qb0, more_item = L + stride < total, last = !more_pass && !more_item;
        SwaItem itn = it; int passn = pass + 1, Ln = L;
        if (!more_pass) { passn = 0; Ln = more_item ? L + stride : L; itn = swa_decode(Ln, nb, nh, nhkv, nqb, nx, nramp, order); }
        const BlockRef<TIn, TOut> nxt = last ? cur : swa_ref<TIn, TOut>(itn, passn, Q, K, V, O, nh, nhkv, sq, skv, qoff);
        causal_swa_block<TIn, TOut>(cur, nxt, skv, W, lds, S);
        if (last) break;
        cur = nxt; it = itn; pass = passn; L = Ln;
    }
}

namespace at2 {
constexpr int LDSB = 131072 + 4096 + 256 + 16384 + 256;
}
#define LDS3 __attribute__((address_space(3)))
typedef short v4s __attribute__((ext_vector_type(4)));
__device__ __forceinline__ unsigned cvtpk_(float lo, float hi) {
    unsigned r; asm("v_cvt_pk_bf16_f32 %0, %1, %2" : "=v"(r) : "v"(lo), "v"(hi)); return r;
}
__device__ __forceinline__ float fma1_(float a, float c, float b) { float r; asm("v_fma_f32 %0, %1, %2, %3" : "=v"(r) : "v"(a), "s"(c), "v"(b)); return r; }
__device__ __forceinline__ float mul1_(float a, float b) { float r; asm("v_mul_f32 %0, %1, %2" : "=v"(r) : "v"(a), "v"(b)); return r; }
__device__ __forceinline__ float sub1_(float a, float b) { float r; asm("v_sub_f32 %0, %1, %2" : "=v"(r) : "v"(a), "v"(b)); return r; }
__device__ __forceinline__ float max3_(float a, float b, float c) { float r; asm("v_max3_f32 %0, %1, %2, %3" : "=v"(r) : "v"(a), "v"(b), "v"(c)); return r; }
__device__ __forceinline__ float max2_(float a, float b) { float r; asm("v_max_f32 %0, %1, %2" : "=v"(r) : "v"(a), "v"(b)); return r; }
__device__ __forceinline__ float xhalf_max(float v) { auto rr = __builtin_amdgcn_permlane32_swap(__float_as_uint(v), __float_as_uint(v), false, false); return fmaxf(__uint_as_float(rr[0]), __uint_as_float(rr[1])); }
constexpr float QSC = 1.4426950408889634f * SCALE, GUARD = 64.f;
__device__ __forceinline__ int lane_volatile() { int r; asm volatile("v_mbcnt_lo_u32_b32 %0, -1, 0\n\tv_mbcnt_hi_u32_b32 %0, -1, %0" : "=v"(r)); return r; }
template <int KB>
__device__ __forceinline__ void qkt2(f32x16& p0, f32x16& p1, const char* Kg, int r32, int hi, const bf16x8* qr) {
    p0 = f32x16{}; p1 = f32x16{};
    const char* kb[4];
#pragma unroll
    for (int dd = 0; dd < 4; ++dd) kb[dd] = Kg + KB * SHM_K + KSWZ(r32, (dd * 16 + hi * 8) * 2);
#pragma unroll
    for (int d0 = 0; d0 < 8; ++d0) { const char* a = kb[d0 & 3] + (d0 >> 2) * 128;
        bf16x8 b0 = *reinterpret_cast<const bf16x8*>(a);
        bf16x8 b1 = *reinterpret_cast<const bf16x8*>(a + 32 * 256);
        p0 = __builtin_amdgcn_mfma_f32_32x32x16_bf16(b0, qr[d0], p0, 0, 0, 0);
        p1 = __builtin_amdgcn_mfma_f32_32x32x16_bf16(b1, qr[d0], p1, 0, 0, 0); }
}
__device__ __forceinline__ void partialSM6(f32x16& p0, f32x16& p1, float& m_reg, float& alpha) {
    float pmax = p0[0];
#pragma unroll
    for (int r = 1; r < 16; ++r) pmax = fmaxf(pmax, p0[r]);
#pragma unroll
    for (int r = 0; r < 16; ++r) pmax = fmaxf(pmax, p1[r]);
    { auto rr = __builtin_amdgcn_permlane32_swap(__float_as_uint(pmax), __float_as_uint(pmax), false, false);
      pmax = fmaxf(__uint_as_float(rr[0]), __uint_as_float(rr[1])); }
    constexpr float C2 = 1.4426950408889634f * SCALE;
    const float mn = fmaxf(m_reg, pmax); alpha = __builtin_amdgcn_exp2f((m_reg - mn) * C2); m_reg = mn;
    const float mnL = -mn * C2;
#pragma unroll
    for (int r = 0; r < 16; ++r) p0[r] = fmaf(p0[r], C2, mnL);
#pragma unroll
    for (int r = 0; r < 16; ++r) p1[r] = fmaf(p1[r], C2, mnL);
#pragma unroll
    for (int r = 0; r < 16; ++r) p0[r] = __builtin_amdgcn_exp2f(p0[r]);
}
__device__ __forceinline__ bf16x8 pk8_(const f32x16& P, int b) {
    u32x4 w = {cvtpk_(P[b + 0], P[b + 1]), cvtpk_(P[b + 2], P[b + 3]), cvtpk_(P[b + 4], P[b + 5]), cvtpk_(P[b + 6], P[b + 7])};
    return *reinterpret_cast<bf16x8*>(&w);
}
__device__ __forceinline__ void finishSM6(f32x16& p0, f32x16& p1, float alpha, float& l_reg, bf16x8* pa) {
#pragma unroll
    for (int r = 0; r < 16; ++r) p1[r] = __builtin_amdgcn_exp2f(p1[r]);
    float ps = 0;
#pragma unroll
    for (int r = 0; r < 16; ++r) ps += p0[r];
#pragma unroll
    for (int r = 0; r < 16; ++r) ps += p1[r];
    { auto rr = __builtin_amdgcn_permlane32_swap(__float_as_uint(ps), __float_as_uint(ps), false, false);
      ps = __uint_as_float(rr[0]) + __uint_as_float(rr[1]); }
    l_reg = l_reg * alpha + ps;
    pa[0] = pk8_(p0, 0); pa[1] = pk8_(p0, 8); pa[2] = pk8_(p1, 0); pa[3] = pk8_(p1, 8);
}
template <int VB>
__device__ __forceinline__ void pv6(f32x16* o, const LDS3 char* vb, const bf16x8* pa) {
#pragma unroll
    for (int ks = 0; ks < 4; ++ks)
#pragma unroll
        for (int d0 = 0; d0 < 4; ++d0) {
            const v4s lo = __builtin_amdgcn_ds_read_tr16_b64_v4i16((LDS3 v4s*)(vb + VB * 16384 + d0 * 512 + ks * 4096));
            const v4s hi = __builtin_amdgcn_ds_read_tr16_b64_v4i16((LDS3 v4s*)(vb + VB * 16384 + d0 * 512 + ks * 4096 + 2048));
            o[d0] = __builtin_amdgcn_mfma_f32_32x32x16_bf16((bf16x8){lo[0], lo[1], lo[2], lo[3], hi[0], hi[1], hi[2], hi[3]}, pa[ks], o[d0], 0, 0, 0);
        }
}
__device__ __forceinline__ float bfl(unsigned u) { return __uint_as_float(u << 16); }
__device__ __forceinline__ float bfh(unsigned u) { return __uint_as_float(u & 0xffff0000u); }
__device__ __forceinline__ unsigned short f2bf_(float f) { unsigned u = __float_as_uint(f); u += 0x7fffu + ((u >> 16) & 1u); return (unsigned short)(u >> 16); }
__device__ __forceinline__ float rbf_(float f) { return __uint_as_float(((unsigned)f2bf_(f)) << 16); }
__device__ __forceinline__ unsigned offb_(unsigned row, unsigned ch) { return 256u * row + 16u * (ch ^ (((row & 3) << 2) | ((row >> 2) & 3))); }

#define RTS(k,wg,i)
#define RTCK(wg,i)
#define RTS2(i)
__global__ __launch_bounds__(512) void pre_kernel(const unsigned short* __restrict__ x, unsigned short* __restrict__ xn, unsigned* __restrict__ cnt,
                                                   const unsigned short* __restrict__ w1, float* __restrict__ cs,
                                                   const unsigned short* __restrict__ ckv, unsigned short* __restrict__ ckp) {
    __shared__ float red[256];
    const int lane = threadIdx.x & 63;
    if (blockIdx.x < 43) {
        u32x4 wv[4];
#pragma unroll
        for (int i = 0; i < 4; ++i) wv[i] = *(const u32x4*)(w1 + (size_t)((threadIdx.x >> 2) + 128 * i) * 1376 + blockIdx.x * 32 + (lane & 3) * 8);
        float c[8], d[4], e[2];
#pragma unroll
        for (int i = 0; i < 4; ++i) {
            c[2 * i] = (__uint_as_float(wv[0][i] << 16) + __uint_as_float(wv[1][i] << 16)) + (__uint_as_float(wv[2][i] << 16) + __uint_as_float(wv[3][i] << 16));
            c[2 * i + 1] = (__uint_as_float(wv[0][i] & 0xffff0000u) + __uint_as_float(wv[1][i] & 0xffff0000u)) + (__uint_as_float(wv[2][i] & 0xffff0000u) + __uint_as_float(wv[3][i] & 0xffff0000u)); }
        const bool b0 = lane & 4, b1 = lane & 8, b2 = lane & 16;
#pragma unroll
        for (int i = 0; i < 4; ++i) d[i] = (b0 ? c[4 + i] : c[i]) + __shfl_xor(b0 ? c[i] : c[4 + i], 4);
#pragma unroll
        for (int i = 0; i < 2; ++i) e[i] = (b1 ? d[2 + i] : d[i]) + __shfl_xor(b1 ? d[i] : d[2 + i], 8);
        float s = (b2 ? e[1] : e[0]) + __shfl_xor(b2 ? e[0] : e[1], 16);
        s += __shfl_xor(s, 32);
        if (lane < 32) red[(threadIdx.x >> 6) * 32 + (lane & 3) * 8 + (lane & 4) + ((lane >> 2) & 2) + (lane >> 4)] = s;
        __syncthreads();
        if (threadIdx.x < 32) { const float* r = red + threadIdx.x;
            cs[blockIdx.x * 32 + threadIdx.x] = ((r[0] + r[32]) + (r[64] + r[96])) + ((r[128] + r[160]) + (r[192] + r[224])); }
        return;
    }
    if (blockIdx.x < 171) {
        const int r = (blockIdx.x - 43) * 16 + (lane >> 2), kt = (threadIdx.x >> 6) * 2;
        const unsigned short* sp = ckv + (size_t)r * 512 + kt * 32 + (lane & 3) * 8;
        const u32x4 c0 = *(const u32x4*)sp, c1 = *(const u32x4*)(sp + 32);
        unsigned short* dp = ckp + ((size_t)kt * 2048 + r) * 32 + (lane & 3) * 8;
        *(u32x4*)dp = c0; *(u32x4*)(dp + 2048 * 32) = c1;
        return;
    }
    const int row = (blockIdx.x - 171) * 8 + (threadIdx.x >> 6);
    const u32x4 v = *(const u32x4*)(x + (size_t)row * 512 + lane * 8);
    if (blockIdx.x == 171 && threadIdx.x < 32) cnt[threadIdx.x * 32] = 0;
    float f[8];
    for (int i = 0; i < 4; ++i) { f[2*i] = __uint_as_float(v[i] << 16); f[2*i+1] = __uint_as_float(v[i] & 0xffff0000u); }
    float sm = 0; for (int i = 0; i < 8; ++i) sm += f[i];
    for (int o = 32; o > 0; o >>= 1) sm += __shfl_xor(sm, o);
    const float mean = sm * (1.f / 512.f);
    float q = 0; for (int i = 0; i < 8; ++i) { const float d = f[i] - mean; q += d * d; }
    for (int o = 32; o > 0; o >>= 1) q += __shfl_xor(q, o);
    const float mb = rbf_(mean), vb = rbf_(q * (1.f / 512.f)), sd = rbf_(sqrtf(rbf_(vb + 1e-5f)));
    u32x4 o; for (int i = 0; i < 4; ++i) o[i] = (unsigned)f2bf_(rbf_(f[2*i] - mb) / sd) | ((unsigned)f2bf_(rbf_(f[2*i+1] - mb) / sd) << 16);
    *(u32x4*)(xn + ((size_t)((row >> 5) * 32 + (lane >> 1)) * 64 + (lane & 1) * 32 + (row & 31)) * 8) = o;
}

namespace sch {
constexpr int nst(int c, int s) { return (int)(((c == 0 ? SCH0 : c == 1 ? SCH1 : c == 2 ? SCH2 : SCH3) >> (4 * s)) & 15u); }
constexpr int nch(int c) { int n = 0; for (int s = 0; s < 8; ++s) n += nst(c, s); return n; }
constexpr int first(int c, int s) { int n = SU0 + SU1 + SU2; for (int i = 0; i < c * 8 + s; ++i) n += nst(i >> 3, i & 7); return n; }
constexpr int last() { int n = SU2 ? 0 : SU1 ? 8 : 20; for (int i = 0; i < 16; ++i) { ++n; if (nst(i >> 3, i & 7)) n = 0; } return n < 8 ? n : 8; }
constexpr int wt(int c) { return c == 0 ? SU0 + 12 + SU1 + 8 + SU2 : c == 1 ? SU1 + 8 + SU2 + 8 + nch(0) : c == 2 ? (PUBC == 2 ? last() : nst(0, 7) + 8 + nch(1)) : PUBC == 2 ? 8 : 0; }
static_assert(first(PUBC, 0) == 16 && (PUBC == 2 || PUBC == 3), "store schedule");
}
__global__ __launch_bounds__(512, 1) void attn2_kernel(const unsigned short* __restrict__ X, const unsigned short* __restrict__ Wq,
                                                       bf16* __restrict__ Qg, bf16* Kgl, bf16* Vgl, bf16* __restrict__ Og,
                                                       const unsigned short* __restrict__ T1, const unsigned short* __restrict__ T2, const unsigned short* __restrict__ T3,
                                                       const unsigned short* __restrict__ Ckv, const unsigned short* __restrict__ Wuk, const unsigned short* __restrict__ Wuv, unsigned* Cnt) {
    extern __shared__ __attribute__((aligned(16))) char lds[];
    const int L = blockIdx.x;
    u32x4 kvw[16];
    const int hd = (L & 7) * 2 + ((L >> 3) & 1) + ((L >> 4) & 1) * 16, x = L >> 5, m0 = (7 - x) * 256;
    RTS(0, L, 0);
    {
        int tid = threadIdx.x; asm volatile("" : "+v"(tid));
        const int wid = __builtin_amdgcn_readfirstlane(tid >> 6), lane = tid & 63, wm = wid >> 2, wn = wid & 3, hi = lane >> 5, l31 = lane & 31;
        const unsigned short* Wp = (wm ? Wuv : Wuk) + hd * 128;
        unsigned aoff[2], woff[2];
#pragma unroll
        for (int i = 0; i < 2; ++i) { const int pos = (wid * 2 + i) * 64 + lane, row = pos >> 2, ch = (pos & 3) ^ ((row >> 2) & 3); aoff[i] = (unsigned)(m0 + row) * 32 + ch * 8; }
#pragma unroll
        for (int i = 0; i < 2; ++i) { const int pos = (wid * 2 + i) * 64 + lane, row = (pos >> 4) & 31,
            ch = (pos & 15) ^ (((row & 3) << 2) | ((row >> 2) & 3)); woff[i] = (unsigned)row * 4096 + ch * 8; }
        f32x16 acc[4][2] = {};
        const unsigned lbase = (unsigned)(size_t)lds;
        const unsigned blk = (lane >> 4) & 1, q4 = (lane & 15) >> 2, p4 = lane & 3;
        const unsigned ard = lbase + (wm * 128 + l31) * 64 + ((hi ^ ((l31 >> 2) & 3)) << 4);
        unsigned brd[2][2];
#pragma unroll
        for (int j = 0; j < 2; ++j) { const int nl = wn * 64 + j * 32, hn = nl >> 7, c = (nl & 127) >> 5;
#pragma unroll
            for (int t = 0; t < 2; ++t) brd[j][t] = lbase + 16384 + hn * 8192 + offb_(8 * hi + 4 * t + q4, 4 * c + 2 * blk + (p4 >> 1)) + 8 * (p4 & 1); }
        bf16x8 a[2][4]; s16x4 bl[2][2], bh[2][2];
#define G2_RD(ks, bo) do { _Pragma("unroll") for (int i = 0; i < 4; ++i) asm volatile("ds_read_b128 %0, %1 offset:%2" : "=&v"(a[ks][i]) : "v"((ard + (bo)) ^ ((ks) << 5)), "i"(i * 2048) : "memory"); \
        _Pragma("unroll") for (int j = 0; j < 2; ++j) { asm volatile("ds_read_b64_tr_b16 %0, %1 offset:%2" : "=&v"(bl[ks][j]) : "v"(brd[j][0] + (bo)), "i"((ks) * 4096) : "memory"); \
            asm volatile("ds_read_b64_tr_b16 %0, %1 offset:%2" : "=&v"(bh[ks][j]) : "v"(brd[j][1] + (bo)), "i"((ks) * 4096) : "memory"); } } while (0)
#define G2_MMH(sl, i0, i1) do { _Pragma("unroll") for (int i = i0; i < i1; ++i) _Pragma("unroll") for (int j = 0; j < 2; ++j) \
        acc[i][j] = __builtin_amdgcn_mfma_f32_32x32x16_bf16((bf16x8){bl[sl][j][0], bl[sl][j][1], bl[sl][j][2], bl[sl][j][3], bh[sl][j][0], bh[sl][j][1], bh[sl][j][2], bh[sl][j][3]}, a[sl][i], acc[i][j], 0, 0, 0); } while (0)
#define G2_PIECE(t, i) do { const int kt = (t) * 32; \
        if ((i) < 2) __builtin_amdgcn_global_load_lds((const unsigned*)(Ckv + kt * 2048 + aoff[(i) & 1]), (unsigned*)(lds + ((t) & 3) * 32768 + (wid * 2 + (i)) * 1024 + lane * 16), 16, 0, 0); \
        else __builtin_amdgcn_global_load_lds((const unsigned*)(Wp + (size_t)kt * 4096 + woff[(i) & 1]), (unsigned*)(lds + ((t) & 3) * 32768 + 16384 + (wid * 2 + (i) - 2) * 1024 + lane * 16), 16, 0, 0); } while (0)
#define LG8() do { asm volatile("s_waitcnt lgkmcnt(8)" ::: "memory"); SBAR(); } while (0)
#define HSTEP(h, VM, LD, LP) do { G2_RD(1, ((h) & 3) * 32768); LG8(); \
            G2_MMH(0, 0, 1); SBAR(); if (LP) G2_PIECE((h) + 3, 2); SBAR(); G2_MMH(0, 1, 3); SBAR(); if (LP) G2_PIECE((h) + 3, 3); SBAR(); G2_MMH(0, 3, 4); SBAR(); \
            asm volatile("s_waitcnt vmcnt(" #VM ") lgkmcnt(0)" ::: "memory"); __builtin_amdgcn_s_barrier(); asm volatile("" ::: "memory"); \
            G2_RD(0, (((h) + 1) & 3) * 32768); SBAR(); \
            G2_MMH(1, 0, 1); SBAR(); if (LD) G2_PIECE((h) + 4, 0); SBAR(); G2_MMH(1, 1, 3); SBAR(); if (LD) G2_PIECE((h) + 4, 1); SBAR(); G2_MMH(1, 3, 4); SBAR(); } while (0)
#pragma unroll
        for (int t = 0; t < 4; ++t)
#pragma unroll
            for (int i = 0; i < 4; ++i) if (t < 3 || i < 2) G2_PIECE(t, i);
        asm volatile("s_waitcnt vmcnt(10)" ::: "memory"); __builtin_amdgcn_s_barrier(); asm volatile("" ::: "memory");
        G2_RD(0, 0); SBAR();
#pragma unroll 1
        for (int h = 0; h < 12; ++h) {
            if ((h & 1) == 0) RTS(0, L, 1 + (h >> 1));
            if (h == 2) RTCK(L, 62);
            HSTEP(h, 8, true, true);
            if (h == WQ_T && L < 128) {
                const int u = (L >> 3) * 512 + tid, hq = (L & 7) * 2 + ((u >> 2) & 1) + ((u >> 3) & 1) * 16;
                __builtin_amdgcn_global_load_lds((const unsigned*)(Wq + (size_t)(u >> 4) * 4096 + hq * 128 + (u & 3) * 32), (unsigned*)(lds + 151808 + lane * 4), 4, 0, 0);
            }
        }
        if (L >= 128 || XN_T == 2) {
#pragma unroll
            for (int i = 0; i < 4 / XN_T; ++i) __builtin_amdgcn_global_load_lds((const unsigned*)(X + ((size_t)((XN_T == 2 ? L : L - 128) >> 3) * (2048 / XN_T) + i * 512 + tid) * 32), (unsigned*)(lds + 151808 + lane * 4), 4, 0, 0);
        }
        RTS(0, L, 7); RTCK(L, 63);
        HSTEP(12, 8, false, true); HSTEP(13, 4, false, false);
        RTS(0, L, 8);
        HSTEP(14, 0, false, false);
        G2_RD(1, 3 * 32768); LG8(); G2_MMH(0, 0, 4); SBAR();
        asm volatile("s_waitcnt lgkmcnt(0)" ::: "memory"); SBAR(); G2_MMH(1, 0, 4); SBAR();
#undef HSTEP
#undef G2_PIECE
#undef G2_MMH
#undef G2_RD
#undef LG8
        RTS(0, L, 9);
        __builtin_amdgcn_s_barrier(); asm volatile("" ::: "memory");
        asm volatile("s_nop 7\n\ts_nop 7" : "+v"(acc[3][0]), "+v"(acc[3][1]));
#pragma unroll
        for (int i = 0; i < 4; ++i)
#pragma unroll
            for (int j = 0; j < 2; ++j)
#pragma unroll
                for (int q = 0; q < 4; ++q) {
                    const int ml = wm * 128 + i * 32 + l31, nl = wn * 64 + j * 32 + 8 * q + 4 * hi;
                    uint2 w; w.x = cvtpk_(acc[i][j][4 * q], acc[i][j][4 * q + 1]); w.y = cvtpk_(acc[i][j][4 * q + 2], acc[i][j][4 * q + 3]);
                    *(uint2*)(lds + ml * 528 + nl * 2) = w; }
        asm volatile("s_waitcnt lgkmcnt(0)" ::: "memory"); __builtin_amdgcn_s_barrier(); asm volatile("" ::: "memory");
        RTS(0, L, 10);
#pragma unroll
        for (int it = 0; it < 16; ++it) kvw[it] = *(const u32x4*)(lds + ((tid >> 5) + 16 * it) * 528 + (tid & 31) * 16);
        asm volatile("s_waitcnt lgkmcnt(0)" ::: "memory"); __builtin_amdgcn_s_barrier(); asm volatile("" ::: "memory");
        RTS(0, L, 11);
    }
    RTS(1, L, 0);
    const size_t hoff = (size_t)hd * 2048 * 128;
    const bf16* K = Kgl + hoff; const bf16* V = Vgl + hoff; bf16* O = Og + hoff;
    int tid = threadIdx.x; asm volatile("" : "+v"(tid));
    const int wid = __builtin_amdgcn_readfirstlane(tid >> 6), lane = tid & 63, r32 = lane & 31, hi = lane >> 5;
    const int g = wid >> 2, rw = wid & 3;
    bf16* Qs = Qg + (size_t)(L * 4 + rw) * 4096;
    if (tid < 64) ((unsigned*)(lds + 131072 + 4096))[tid] = 0;
    const int NA = 16 - x;
    char* Kg = lds + g * 2 * 16384; char* Vg = lds + 65536 + g * 2 * 16384;
    unsigned koff[2], voff[2];
#pragma unroll
    for (int i = 0; i < 2; ++i) { const int r = rw * 16 + i * 4 + (lane >> 4); koff[i] = (r * 128 + (((lane & 15) ^ (r & 7)) << 3)) * 2;
        const int kk = rw * 16 + ((lane >> 2) & 7), c = ((i * 2 + (lane >> 5)) & 3) * 32 + (lane & 3) * 8;
        voff[i] = (kk * 128 + c) * 2; }
    const int P0 = 14 - 2 * x;
#define TILE(s) (2 * ((s) >= NA ? (s) - NA : (s) < 2 ? P0 + (s) : (s) - 2 < P0 ? (s) - 2 : (s)) + g)
    const __amdgpu_buffer_rsrc_t rsK = __builtin_amdgcn_make_buffer_rsrc((void*)K, 0, 2048 * 256, 0x00020000);
    const __amdgpu_buffer_rsrc_t rsV = __builtin_amdgcn_make_buffer_rsrc((void*)V, 0, 2048 * 256, 0x00020000);
#define GLK(s, bf) do { const unsigned so_ = (unsigned)TILE(s) * 16384u; unsigned k1_; asm volatile("v_xor_b32 %0, 64, %1\n\tv_add_u32 %0, 0x400, %0" : "=v"(k1_) : "v"(koff[0])); \
        __builtin_amdgcn_raw_ptr_buffer_load_lds(rsK, (LDS3 void*)(Kg + (bf) * 16384 + (rw * 4) * 1024), 16, koff[0], so_, 0, 0); \
        __builtin_amdgcn_raw_ptr_buffer_load_lds(rsK, (LDS3 void*)(Kg + (bf) * 16384 + (rw * 4) * 1024), 16, koff[0], so_, 2048, 0); \
        __builtin_amdgcn_raw_ptr_buffer_load_lds(rsK, (LDS3 void*)(Kg + (bf) * 16384 + (rw * 4 + 1) * 1024), 16, k1_, so_, 0, 0); \
        __builtin_amdgcn_raw_ptr_buffer_load_lds(rsK, (LDS3 void*)(Kg + (bf) * 16384 + (rw * 4 + 1) * 1024), 16, k1_, so_, 2048, 0); } while (0)
#define GLV(s, bf) do { const unsigned so_ = (unsigned)TILE(s) * 16384u; _Pragma("unroll") for (int i = 0; i < 2; ++i) { \
        __builtin_amdgcn_raw_ptr_buffer_load_lds(rsV, (LDS3 void*)(Vg + (bf) * 16384 + (rw * 4 + i) * 1024), 16, voff[i], so_, 0, 0); \
        __builtin_amdgcn_raw_ptr_buffer_load_lds(rsV, (LDS3 void*)(Vg + (bf) * 16384 + (rw * 4 + i) * 1024), 16, voff[i], so_, 2048, 0); } } while (0)
#define LANE_() lane_volatile()
    bf16x8 qr[8];
    const unsigned* cp = Cnt + hd * 32; unsigned pv;
    {
        const int R = (g ? x : 15 - x) * 4 + rw;
        const unsigned short* xr = X + (size_t)R * 32 * 64 * 8;
        u32x4 xa[2][8];
#define LDX1(c, s) asm volatile("global_load_dwordx4 %0, %1, %2 offset:%3" : "=&v"(xa[(c) & 1][s]) : "v"(lane * 16 + (c) * 8192 + ((s) >> 2) * 4096), "s"(xr), "i"(((s) & 3) * 1024) : "memory")
#define LDX(c) do { _Pragma("unroll") for (int s = 0; s < 8; ++s) LDX1(c, s); SBAR(); } while (0)
#define LDW(c) do { _Pragma("unroll") for (int i = 0; i < 4; ++i) { const int p = (c) * 32 + wid * 4 + i, pos = p * 64 + lane, k = pos >> 4, row = k & 63, ch = (pos & 15) ^ (((row & 3) << 2) | ((row >> 2) & 3)); \
            __builtin_amdgcn_global_load_lds((const unsigned*)(Wq + (size_t)k * 4096 + hd * 128 + ch * 8), (unsigned*)(lds + p * 1024 + lane * 16), 16, 0, 0); } SBAR(); } while (0)
        const __amdgpu_buffer_rsrc_t rsO = __builtin_amdgcn_make_buffer_rsrc((void*)Kgl, 0, 2 * 2048 * 4096 * 2, 0x00020000);
        const unsigned vo = (((((tid >> 4) & 1) * 32 + hd) * 2048 + m0 + (tid >> 5)) * 128 + (tid & 15) * 8) * 2;
#define STO(n0, n) do { _Pragma("unroll") for (int it = (n0); it < (n0) + (n); ++it) __builtin_amdgcn_raw_buffer_store_b128(kvw[it], rsO, vo, it * 4096, 16); \
            SBAR(); if ((n) > 0) asm volatile("s_nop 1" ::: "memory"); SBAR(); } while (0)
        LDX(0); LDW(0); STO(0, SU0); LDX(1); LDW(1); STO(SU0, SU1); LDW(2); LDW(3); STO(SU0 + SU1, SU2);
        const unsigned lbase = (unsigned)(size_t)lds, blk = (lane >> 4) & 1, q4 = (lane & 15) >> 2, p4 = lane & 3;
        unsigned brd[4][2];
#pragma unroll
        for (int j = 0; j < 4; ++j)
#pragma unroll
            for (int t = 0; t < 2; ++t) brd[j][t] = lbase + offb_(8 * hi + 4 * t + q4, 4 * j + 2 * blk + (p4 >> 1)) + 8 * (p4 & 1);
        f32x16 acc[4] = {};
#pragma unroll
        for (int c = 0; c < 4; ++c) {
            asm volatile("s_waitcnt vmcnt(%8)" : "+v"(xa[c & 1][0]), "+v"(xa[c & 1][1]), "+v"(xa[c & 1][2]), "+v"(xa[c & 1][3]), "+v"(xa[c & 1][4]), "+v"(xa[c & 1][5]), "+v"(xa[c & 1][6]), "+v"(xa[c & 1][7]) : "i"(sch::wt(c)) : "memory");
            __builtin_amdgcn_s_barrier(); asm volatile("" ::: "memory");
            RTS(1, L, 1 + c);
            if (c == PUBC) {
                if (tid == 0) __hip_atomic_fetch_add((__attribute__((address_space(1))) unsigned*)(Cnt + hd * 32), 1u, __ATOMIC_RELAXED, __HIP_MEMORY_SCOPE_AGENT);
                GLK(0, 0); GLK(1, 1); }
            SBAR();
            s16x4 bl[2][4], bh[2][4];
#define QRD(s, sl) do { _Pragma("unroll") for (int j = 0; j < 4; ++j) { asm volatile("ds_read_b64_tr_b16 %0, %1 offset:%2" : "=&v"(bl[sl][j]) : "v"(brd[j][0] + c * 32768), "i"((s) * 4096) : "memory"); \
                asm volatile("ds_read_b64_tr_b16 %0, %1 offset:%2" : "=&v"(bh[sl][j]) : "v"(brd[j][1] + c * 32768), "i"((s) * 4096) : "memory"); } } while (0)
            QRD(0, 0);
#pragma unroll
            for (int s = 0; s < 8; ++s) {
                if (s < 7) { QRD(s + 1, (s + 1) & 1); asm volatile("s_waitcnt lgkmcnt(8)" ::: "memory"); } else asm volatile("s_waitcnt lgkmcnt(0)" ::: "memory");
                SBAR();
                const bf16x8 xf = *reinterpret_cast<const bf16x8*>(&xa[c & 1][s]);
#pragma unroll
                for (int j = 0; j < 4; ++j) acc[j] = __builtin_amdgcn_mfma_f32_32x32x16_bf16((bf16x8){bl[s & 1][j][0], bl[s & 1][j][1], bl[s & 1][j][2], bl[s & 1][j][3], bh[s & 1][j][0], bh[s & 1][j][1], bh[s & 1][j][2], bh[s & 1][j][3]}, xf, acc[j], 0, 0, 0);
                SBAR();
                if (c < 2) { LDX1(c + 2, s); SBAR(); }
                STO(sch::first(c, s), sch::nst(c, s));
            }
#undef QRD
        }
        RTS2(40);
        __builtin_amdgcn_s_barrier(); asm volatile("" ::: "memory");
        RTS2(42);
        GLV(0, 0); SBAR();
        RTS2(43);
#undef LDX
#undef LDX1
#undef LDW
#undef STO
#pragma unroll
        for (int j = 0; j < 4; ++j) acc[j] *= QSC;
#pragma unroll
        for (int j = 0; j < 4; ++j)
#pragma unroll
            for (int s2 = 0; s2 < 2; ++s2) { const int B_ = 8 * s2;
                unsigned a0 = cvtpk_(acc[j][B_], acc[j][B_ + 1]), a1 = cvtpk_(acc[j][B_ + 2], acc[j][B_ + 3]), b0 = cvtpk_(acc[j][B_ + 4], acc[j][B_ + 5]), b1 = cvtpk_(acc[j][B_ + 6], acc[j][B_ + 7]);
                asm volatile("s_nop 1" : "+v"(a0), "+v"(a1), "+v"(b0), "+v"(b1));
                auto r0 = __builtin_amdgcn_permlane32_swap(a0, b0, false, false); auto r1 = __builtin_amdgcn_permlane32_swap(a1, b1, false, false);
                u32x4 w = {r0[0], r1[0], r0[1], r1[1]}; qr[2 * j + s2] = *reinterpret_cast<bf16x8*>(&w); }
        char* xq = (rw < 2 ? lds + 114688 + rw * 8192 : lds + 135424 + (rw - 2) * 8192) + lane * 16;
        RTS2(44);
        asm volatile("s_waitcnt vmcnt(8)" ::: "memory");
        if (g == 0) {
#pragma unroll
            for (int d0 = 0; d0 < 8; ++d0) *(bf16x8*)(xq + d0 * 1024) = qr[d0];
        } else {
#pragma unroll
            for (int d0 = 0; d0 < 8; ++d0) *(bf16x8*)(Qs + d0 * 512 + lane * 8) = qr[d0];
        }
        RTS2(45);
        asm volatile("global_load_dword %0, %1, %2 sc1" : "=&v"(pv) : "v"(0), "s"(cp) : "memory");
        asm volatile("s_waitcnt lgkmcnt(0)" ::: "memory"); __builtin_amdgcn_s_barrier(); asm volatile("" ::: "memory");
        RTS2(46);
        if (g == 1) {
#pragma unroll
            for (int d0 = 0; d0 < 8; ++d0) qr[d0] = *(const bf16x8*)(xq + d0 * 1024);
        }
    }
    RTS(1, L, 5);
    int qlo = (15 - x) * 128 + rw * 32;
    float nref = 0.f, nsave = 0.f, lA = 0.f, lB = 0.f; f32x16 o[4] = {}, cin = {};
    const LDS3 char* vb0 = (const LDS3 char*)(Vg + v_rd_base(lane));
    LDS3 unsigned* cnt3 = (LDS3 unsigned*)(lds + 131072 + 4096 + g * 64);
    unsigned gcnt = 0;
    LDS3 unsigned* cnt3o = (LDS3 unsigned*)(lds + 131072 + 4096 + (g ^ 1) * 64);
#define GBAR() GBARW(asm volatile("s_waitcnt vmcnt(0) lgkmcnt(0)" ::: "memory"))
#define GBARW(W_) do { W_; gcnt += 4;                                   \
        if (LANE_() == 0) __hip_atomic_fetch_add(cnt3, 1u, __ATOMIC_RELAXED, __HIP_MEMORY_SCOPE_WORKGROUP);                      \
        while ((unsigned)__builtin_amdgcn_readfirstlane(*(volatile LDS3 unsigned*)cnt3) < gcnt) __builtin_amdgcn_s_sleep(1);  \
        PRIO_();                                                                                                              \
        asm volatile("" ::: "memory"); } while (0)
#define PRIO_() do { if ((unsigned)__builtin_amdgcn_readfirstlane(*(volatile LDS3 unsigned*)cnt3o) + AT_PRIO >= gcnt) __builtin_amdgcn_s_setprio(1); else __builtin_amdgcn_s_setprio(0); } while (0)
#define SGB(m, n) __builtin_amdgcn_sched_group_barrier(m, n, 0)
#define SGB_A() do { SGB(0x100, 4); _Pragma("unroll") for (int i_ = 0; i_ < 12; ++i_) { SGB(0x8, 1); SGB(0x100, 1); SGB(0x402, 7); } \
                     _Pragma("unroll") for (int i_ = 0; i_ < 4; ++i_) { SGB(0x8, 1); SGB(0x402, 7); } } while (0)
#define SGB_B() do { SGB(0x100, 8); _Pragma("unroll") for (int i_ = 0; i_ < 12; ++i_) { SGB(0x8, 1); SGB(0x100, 2); SGB(0x402, 5); } \
                     _Pragma("unroll") for (int i_ = 0; i_ < 4; ++i_) { SGB(0x8, 1); SGB(0x402, 5); } } while (0)
#define RESC6(a) do { _Pragma("unroll") for (int d_ = 0; d_ < 4; ++d_) o[d_] *= (a); } while (0)
#define MOUT(KA, j) do { f32x4 rv[4]; _Pragma("unroll") for (int i = 0; i < 4; ++i) rv[i] = sp[((j) * 4 + i) * 64];               \
          _Pragma("unroll") for (int qp = 0; qp < 2; ++qp) { float v[8];                                                      \
              _Pragma("unroll") for (int e = 0; e < 8; ++e) v[e] = KA[8 * qp + e] * w0 + rv[qp * 2 + (e >> 2)][e & 3] * w1;     \
              unsigned ax = cvtpk_(v[0], v[1]), ay = cvtpk_(v[2], v[3]), bx = cvtpk_(v[4], v[5]), by = cvtpk_(v[6], v[7]); \
              asm volatile("s_nop 1" : "+v"(ax), "+v"(ay), "+v"(bx), "+v"(by));                                                \
              auto r0 = __builtin_amdgcn_permlane32_swap(ax, bx, false, false); auto r1 = __builtin_amdgcn_permlane32_swap(ay, by, false, false); \
              *(u32x4*)(Ow + (j) * 32 + qp * 16) = (u32x4){r0[0], r1[0], r0[1], r1[1]}; } } while (0)
#define MSEND(A_, B_) do { _Pragma("unroll") for (int i = 0; i < 4; ++i) { dp[i * 64] = (f32x4){A_[4 * i], A_[4 * i + 1], A_[4 * i + 2], A_[4 * i + 3]};        \
                                                              dp[(4 + i) * 64] = (f32x4){B_[4 * i], B_[4 * i + 1], B_[4 * i + 2], B_[4 * i + 3]}; } } while (0)
#define MERGE(KBF, MM_, QL_) do {                                                                                                       \
        ONOP(); GBAR(); int ln = LANE_(); asm volatile("" : "+v"(ln));                                                           \
        { char* my = (rw < 2) ? Kg + (KBF) * 16384 + rw * 8192 : Vg + ((KBF) ^ 1) * 16384 + (rw - 2) * 8192;                  \
          f32x4* dp = (f32x4*)(my + ln * 16);                                                                               \
          if (g == 0) MSEND(o[2], o[3]); else MSEND(o[0], o[1]);                                                              \
          { float ps = lA + lB; auto rr = __builtin_amdgcn_permlane32_swap(__float_as_uint(ps), __float_as_uint(ps), false, false); lA = __uint_as_float(rr[0]) + __uint_as_float(rr[1]); } \
          float* mlp = (float*)(lds + 131072) + wid * 128; mlp[ln] = (MM_); mlp[64 + ln] = lA; }                       \
        asm volatile("s_waitcnt lgkmcnt(0)" ::: "memory"); __builtin_amdgcn_s_barrier(); asm volatile("" ::: "memory");      \
        { char* Kp = lds + (1 - g) * 32768; char* Vp = lds + 65536 + (1 - g) * 32768;                                         \
          const char* pr = (rw < 2) ? Kp + (KBF) * 16384 + rw * 8192 : Vp + ((KBF) ^ 1) * 16384 + (rw - 2) * 8192;            \
          const f32x4* sp = (const f32x4*)(pr + ln * 16);                                                                   \
          const float* mlq = (const float*)(lds + 131072) + (wid ^ 4) * 128; const float m2 = mlq[ln], l2 = mlq[64 + ln]; \
          const float mm = fminf((MM_), m2);                                                                                  \
          const float a0 = __builtin_amdgcn_exp2f(mm - (MM_)), a1 = __builtin_amdgcn_exp2f(mm - m2);                          \
          const float rl = __builtin_amdgcn_rcpf(lA * a0 + l2 * a1), w0 = a0 * rl, w1 = a1 * rl;                              \
          bf16* Ow = O + (size_t)((QL_) + (ln & 31)) * D + g * 64 + 8 * (ln >> 5);                                                           \
          if (g == 0) { MOUT(o[0], 0); SBAR(); MOUT(o[1], 1); } else { MOUT(o[2], 0); SBAR(); MOUT(o[3], 1); } }              \
        asm volatile("s_waitcnt lgkmcnt(0)" ::: "memory"); __builtin_amdgcn_s_barrier(); asm volatile("" ::: "memory");      \
    } while (0)
    f32x16 pA0, pA1, pB0, pB1;
    int kad[4];
#pragma unroll
    for (int dd = 0; dd < 4; ++dd) kad[dd] = (int)(uintptr_t)Kg + KSWZ(r32, (dd * 16 + hi * 8) * 2);
    const int vad = (int)(uintptr_t)Vg + v_rd_base(lane);
    s16x4 vl[4], vh[4];
    bf16x8 kf[4];
#define LGW(n) asm volatile("s_waitcnt lgkmcnt(%0)" :: "i"(n) : "memory")
#define VRD(VB, j) do { asm volatile("ds_read_b64_tr_b16 %0, %1 offset:%2" : "=&v"(vl[(j) & 3]) : "v"(vad), "i"((VB) * 16384 + ((j) & 3) * 512 + ((j) >> 2) * 4096) : "memory"); \
                        asm volatile("ds_read_b64_tr_b16 %0, %1 offset:%2" : "=&v"(vh[(j) & 3]) : "v"(vad), "i"((VB) * 16384 + ((j) & 3) * 512 + ((j) >> 2) * 4096 + 2048) : "memory"); } while (0)
#define KRD(KB, i) asm volatile("ds_read_b128 %0, %1 offset:%2" : "=&v"(kf[(i) & 3]) : "v"(kad[((i) >> 1) & 3]), "i"((KB) * 16384 + ((i) >> 3) * 128 + ((i) & 1) * 8192) : "memory")
#define GLP(KB, i) do {                                                                                                       \
        if ((i) == 0) __builtin_amdgcn_raw_ptr_buffer_load_lds(rsK, (LDS3 void*)(Kg + ((KB) ^ 1) * 16384 + (rw * 4) * 1024), 16, koff[0], sk_, 0, 0);        \
        if ((i) == 1) __builtin_amdgcn_raw_ptr_buffer_load_lds(rsK, (LDS3 void*)(Kg + ((KB) ^ 1) * 16384 + (rw * 4) * 1024), 16, koff[0], sk_, 2048, 0);     \
        if ((i) == 2) { asm volatile("v_xor_b32 %0, 64, %1\n\tv_add_u32 %0, 0x400, %0" : "=v"(t1_) : "v"(koff[0]));              \
                        __builtin_amdgcn_raw_ptr_buffer_load_lds(rsK, (LDS3 void*)(Kg + ((KB) ^ 1) * 16384 + (rw * 4 + 1) * 1024), 16, t1_, sk_, 0, 0); }    \
        if ((i) == 3) __builtin_amdgcn_raw_ptr_buffer_load_lds(rsK, (LDS3 void*)(Kg + ((KB) ^ 1) * 16384 + (rw * 4 + 1) * 1024), 16, t1_, sk_, 2048, 0);     \
        if ((i) == 4) __builtin_amdgcn_raw_ptr_buffer_load_lds(rsV, (LDS3 void*)(Vg + (KB) * 16384 + (rw * 4) * 1024), 16, voff[0], sv_, 0, 0);              \
        if ((i) == 5) __builtin_amdgcn_raw_ptr_buffer_load_lds(rsV, (LDS3 void*)(Vg + (KB) * 16384 + (rw * 4) * 1024), 16, voff[0], sv_, 2048, 0);           \
        if ((i) == 6) { asm volatile("v_add_u32 %0, 0x80, %1" : "=v"(t1_) : "v"(voff[0]));                                    \
                        __builtin_amdgcn_raw_ptr_buffer_load_lds(rsV, (LDS3 void*)(Vg + (KB) * 16384 + (rw * 4 + 1) * 1024), 16, t1_, sv_, 0, 0); }          \
        if ((i) == 7) __builtin_amdgcn_raw_ptr_buffer_load_lds(rsV, (LDS3 void*)(Vg + (KB) * 16384 + (rw * 4 + 1) * 1024), 16, t1_, sv_, 2048, 0); } while (0)
#define DOT2(l, pk) asm volatile("v_dot2_f32_bf16 %0, %1, %2, %0" : "+v"(l) : "v"(pk), "s"(0x3f803f80u))
#define SMFIN(PY0, PY1, i) do {                                                                                               \
        if ((i) < 10) PY1[6 + (i)] = __builtin_amdgcn_exp2f(PY1[6 + (i)]);                                                     \
        if ((i) < 8) { const unsigned w_ = cvtpk_(PY0[2 * (i)], PY0[2 * (i) + 1]); PY0[(i)] = __uint_as_float(w_); DOT2(lA, w_); }        \
        else { const unsigned w_ = cvtpk_(PY1[2 * ((i) - 8)], PY1[2 * ((i) - 8) + 1]); PY0[(i)] = __uint_as_float(w_); DOT2(lB, w_); } } while (0)
#define RESC4(i, a) do { f32x4 t4 = {mul1_(o[(i) >> 2][4 * ((i) & 3)], a), mul1_(o[(i) >> 2][4 * ((i) & 3) + 1], a), mul1_(o[(i) >> 2][4 * ((i) & 3) + 2], a), mul1_(o[(i) >> 2][4 * ((i) & 3) + 3], a)}; \
        o[(i) >> 2][4 * ((i) & 3)] = t4[0]; o[(i) >> 2][4 * ((i) & 3) + 1] = t4[1]; o[(i) >> 2][4 * ((i) & 3) + 2] = t4[2]; o[(i) >> 2][4 * ((i) & 3) + 3] = t4[3]; \
        asm volatile("" : "+v"(o[(i) >> 2])); } while (0)
#define QKPH(PX0, PX1, PY0, PY1, s, KB) do {                                                                                  \
        const unsigned sk_ = (unsigned)TILE((s) < 16 ? (s) + 1 : 16) * 16384u, sv_ = (unsigned)TILE(s) * 16384u; unsigned t1_; \
        SBAR(); KRD(KB, 0); KRD(KB, 1); KRD(KB, 2); KRD(KB, 3);                                                               \
        _Pragma("unroll") for (int i = 0; i < 16; ++i) {                                                                      \
            LGW(i < 13 ? 3 : i - 9); SBAR();                                                                                  \
            if (i == 0) PX0 = __builtin_amdgcn_mfma_f32_32x32x16_bf16(kf[0], qr[0], cin, 0, 0, 0);                            \
            else if (i == 1) PX1 = __builtin_amdgcn_mfma_f32_32x32x16_bf16(kf[1], qr[0], cin, 0, 0, 0);                       \
            else if ((i & 1) == 0) PX0 = __builtin_amdgcn_mfma_f32_32x32x16_bf16(kf[i & 3], qr[i >> 1], PX0, 0, 0, 0);        \
            else PX1 = __builtin_amdgcn_mfma_f32_32x32x16_bf16(kf[i & 3], qr[i >> 1], PX1, 0, 0, 0);                          \
            SBAR();                                                                                                           \
            if (i < 12) KRD(KB, i + 4); else VRD((KB) ^ 1, i - 12);                                                           \
            if ((i & 1) == 0) GLP(KB, i >> 1);                                                                                \
            SMFIN(PY0, PY1, i);                                                                                               \
            SBAR();                                                                                                           \
        } } while (0)
#define PVMM(PP, j) asm volatile("v_mfma_f32_32x32x16_bf16 %0, %1, %2, %0" : "+v"(o[(j) & 3]) : "v"((bf16x8){vl[(j) & 3][0], vl[(j) & 3][1], vl[(j) & 3][2], vl[(j) & 3][3], vh[(j) & 3][0], vh[(j) & 3][1], vh[(j) & 3][2], vh[(j) & 3][3]}), \
            "v"((f32x4){PP[4 * ((j) >> 2)], PP[4 * ((j) >> 2) + 1], PP[4 * ((j) >> 2) + 2], PP[4 * ((j) >> 2) + 3]}))
#define ONOP() asm volatile("s_nop 7\n\ts_nop 7\n\ts_nop 3" : "+v"(o[0]), "+v"(o[1]), "+v"(o[2]), "+v"(o[3]))
#define CSET(k) asm volatile("v_mov_b32 %0, %1" : "=v"(cin[k]) : "v"(nref))
#define PVPH(VB, MM, PX0, PX1, PP, ODD, FIRST) do {                                                                           \
        float ma_ = (FIRST) ? -3.0e38f : 0.f, mb_ = -3.0e38f, ex_ = 1.f; bool pend_ = false;                                  \
        _Pragma("unroll") for (int j = 0; j < 16; ++j) {                                                                      \
            if (MM) { LGW(j < 13 ? 6 : 2 * (15 - j)); SBAR(); PVMM(PP, j); SBAR(); if (j < 12) VRD(VB, j + 4); }              \
            if (j < 2) { ma_ = max3_(ma_, PX0[8 * j], PX0[8 * j + 1]); mb_ = max3_(mb_, PX0[8 * j + 2], PX0[8 * j + 3]);     \
                         ma_ = max3_(ma_, PX0[8 * j + 4], PX0[8 * j + 5]); mb_ = max3_(mb_, PX0[8 * j + 6], PX0[8 * j + 7]); \
                         asm volatile("" : "+v"(ma_), "+v"(mb_)); }                                                           \
            else if (j < 4) { ma_ = max3_(ma_, PX1[8 * j - 16], PX1[8 * j - 15]); mb_ = max3_(mb_, PX1[8 * j - 14], PX1[8 * j - 13]); \
                         ma_ = max3_(ma_, PX1[8 * j - 12], PX1[8 * j - 11]); mb_ = max3_(mb_, PX1[8 * j - 10], PX1[8 * j - 9]); \
                         asm volatile("" : "+v"(ma_), "+v"(mb_)); }                                                           \
            else if (j == 4) {                                                                                                \
                const float mrun = max2_(ma_, mb_);                                                                           \
                if ((FIRST) || __builtin_amdgcn_ballot_w64(mrun > GUARD) != 0) {                                              \
                    const float d_ = fmaxf(xhalf_max(mrun), -1.0e30f);                                                        \
                    _Pragma("unroll") for (int r = 0; r < 16; ++r) { PX0[r] -= d_; PX1[r] -= d_; }                            \
                    nsave = nref; nref -= d_;                                                                                 \
                    _Pragma("unroll") for (int r = 0; r < 16; ++r) CSET(r);                                                   \
                    if (!(FIRST)) { ex_ = __builtin_amdgcn_exp2f(-d_); lA *= ex_; lB *= ex_; pend_ = true; } } }              \
            if (j >= 5 && j < 13) { PX0[2 * (j - 5)] = __builtin_amdgcn_exp2f(PX0[2 * (j - 5)]); PX0[2 * (j - 5) + 1] = __builtin_amdgcn_exp2f(PX0[2 * (j - 5) + 1]); \
                                    asm volatile("" : "+v"(PX0)); }                                                           \
            if (j >= 13) { PX1[2 * (j - 13)] = __builtin_amdgcn_exp2f(PX1[2 * (j - 13)]); PX1[2 * (j - 13) + 1] = __builtin_amdgcn_exp2f(PX1[2 * (j - 13) + 1]); \
                                    asm volatile("" : "+v"(PX1)); }                                                           \
            SBAR();                                                                                                           \
        }                                                                                                                     \
        if (pend_) { ONOP(); _Pragma("unroll") for (int i = 0; i < 16; ++i) RESC4(i, ex_); } } while (0)
#define WARM() do { \
            const unsigned vo = ((unsigned)(L * 8 + wid) * 64u + (unsigned)LANE_()) * 64u; \
            __builtin_amdgcn_raw_ptr_buffer_load_lds(__builtin_amdgcn_make_buffer_rsrc((void*)T1, 0, 4096 * 512 * 2, 0x00020000), (LDS3 void*)(lds + 151808), 4, vo, 0, 0, 0); \
            __builtin_amdgcn_raw_ptr_buffer_load_lds(__builtin_amdgcn_make_buffer_rsrc((void*)T2, 0, 1376 * 512 * 2, 0x00020000), (LDS3 void*)(lds + 151808), 4, vo - 4194304u, 0, 0, 0); \
            __builtin_amdgcn_raw_ptr_buffer_load_lds(__builtin_amdgcn_make_buffer_rsrc((void*)T3, 0, 1376 * 512 * 2, 0x00020000), (LDS3 void*)(lds + 151808), 4, vo - 5603328u, 0, 0, 0); \
        } while (0)
    SBAR(); qkt2<0>(pA0, pA1, Kg, r32, hi, qr);
    asm volatile("s_nop 7\n\ts_nop 7" : "+v"(pA0), "+v"(pA1));
    asm volatile("s_waitcnt vmcnt(0)" : "+v"(pv) :: "memory");
    const bool rdy = (unsigned)__builtin_amdgcn_readfirstlane(pv) >= 8u;
    if (rw == 0 && rdy) asm volatile("buffer_inv sc1" ::: "memory");
    SBAR();
    PVPH(0, false, pA0, pA1, pA0, false, true);
    RTS2(41);
    if (rw == 0 && !rdy) {
        for (unsigned sp = 0; sp < (1u << 20) && (unsigned)__builtin_amdgcn_readfirstlane(pv) < 8u; ++sp)
            pv = __hip_atomic_load((const __attribute__((address_space(1))) unsigned*)cp, __ATOMIC_RELAXED, __HIP_MEMORY_SCOPE_AGENT);
        RTS2(47);
        __builtin_amdgcn_fence(__ATOMIC_ACQUIRE, "agent");
    }
    GBAR();
    RTS(1, L, 6); RTCK(L, 60);
#define STEP6(PX0, PX1, PY0, PY1, s, KB) do {                                                                                 \
        QKPH(PX0, PX1, PY0, PY1, s, KB);                                                                                      \
        if ((s) == NA - 1) { SBAR(); while ((unsigned)__builtin_amdgcn_readfirstlane(*(volatile LDS3 unsigned*)(lds + 131072 + 4096 + 64)) < 4u) __builtin_amdgcn_s_sleep(1); int ln = LANE_(); asm volatile("" : "+v"(ln)); _Pragma("unroll") for (int d0 = 0; d0 < 8; ++d0) qr[d0] = *(const bf16x8*)(Qs + d0 * 512 + ln * 8); } \
        const int qA_ = qlo; if ((s) == NA) qlo = x * 128 + rw * 32;                                                          \
        if ((s) == NA - 1 || (s) == 16) { const int kb_ = TILE(s) * 64; if (kb_ + 63 > qlo) { int ln = LANE_(); asm volatile("" : "+v"(ln)); mask_tile(PX0, PX1, qlo + (ln & 31) - 4 * (ln >> 5) - kb_, 0x7fffffffu); } } \
        PVPH((KB) ^ 1, true, PX0, PX1, PY0, KB, (s) == NA);                                                                   \
        if ((s) == NA) { MERGE(KB, nsave, qA_); lA = 0.f; lB = 0.f; _Pragma("unroll") for (int d_ = 0; d_ < 4; ++d_) o[d_] = f32x16{}; } \
        GBAR(); RTS(1, L, 6 + (s)); } while (0)
    {
        asm volatile("" : "+v"(o[0]), "+v"(o[1]), "+v"(o[2]), "+v"(o[3]), "+v"(lA), "+v"(lB));
        QKPH(pB0, pB1, pA0, pA1, 1, 1);
        if (WARM_S == 1) WARM();
        if (x == 0) { const int kb_ = TILE(1) * 64; if (kb_ + 63 > qlo) { int ln = LANE_(); asm volatile("" : "+v"(ln)); mask_tile(pB0, pB1, qlo + (ln & 31) - 4 * (ln >> 5) - kb_, 0x7fffffffu); } }
        PVPH(0, true, pB0, pB1, pA0, 1, false);
        if (WARM_S == 1) GBARW(asm volatile("s_waitcnt vmcnt(3) lgkmcnt(0)" ::: "memory")); else GBAR();
        RTS(1, L, 7);
        QKPH(pA0, pA1, pB0, pB1, 2, 0);
        if (WARM_S == 2) WARM();
        PVPH(1, true, pA0, pA1, pB0, 0, false);
        if (WARM_S == 2) GBARW(asm volatile("s_waitcnt vmcnt(3) lgkmcnt(0)" ::: "memory")); else GBAR();
        RTS(1, L, 8);
    }
#pragma unroll 1
    for (int s = 3; s < 17; s += 2) {
        STEP6(pB0, pB1, pA0, pA1, s, 1);
        STEP6(pA0, pA1, pB0, pB1, s + 1, 0);
    }
    SBAR(); VRD(0, 0); VRD(0, 1); VRD(0, 2); VRD(0, 3);
#pragma unroll
    for (int i = 0; i < 16; ++i) { SMFIN(pA0, pA1, i); SBAR(); }
    SBAR();
#pragma unroll
    for (int j = 0; j < 16; ++j) { LGW(j < 13 ? 6 : 2 * (15 - j)); SBAR(); PVMM(pA0, j); SBAR(); if (j < 12) VRD(0, j + 4); }
    MERGE(1, nref, qlo);
#undef DOT2
#undef SMFIN
#undef RESC4
#undef PVMM
#undef ONOP
#undef CSET
#undef TILE
#undef LANE_
#undef GLK
#undef GLV
#undef GBAR
#undef PRIO_
#undef RESC6
#undef SGB
#undef SGB_A
#undef SGB_B
#undef MERGE
#undef MOUT
#undef MSEND
#undef STEP6
#undef LGW
#undef VRD
#undef PVPH
#undef KRD
#undef GLP
#undef QKPH
}

typedef short s16x8 __attribute__((ext_vector_type(8)));
__device__ __forceinline__ float bf2f(unsigned short u) { return __uint_as_float(((unsigned)u) << 16); }
__device__ __forceinline__ unsigned short f2bf(float f) { unsigned u = __float_as_uint(f); u += 0x7fffu + ((u >> 16) & 1u); return (unsigned short)(u >> 16); }
__device__ __forceinline__ float rbf(float f) { return bf2f(f2bf(f)); }

__global__ __launch_bounds__(256) void ln_kernel(const unsigned short* __restrict__ x, unsigned short* __restrict__ y, int rows) {
    int row = blockIdx.x * 4 + (threadIdx.x >> 6); int lane = threadIdx.x & 63;
    if (row >= rows) return;
    const u32x4 v = *(const u32x4*)(x + (size_t)row * 512 + lane * 8);
    float f[8];
    for (int i = 0; i < 4; ++i) { f[2*i] = __uint_as_float(v[i] << 16); f[2*i+1] = __uint_as_float(v[i] & 0xffff0000u); }
    float s = 0; for (int i = 0; i < 8; ++i) s += f[i];
    for (int o = 32; o > 0; o >>= 1) s += __shfl_xor(s, o);
    float mean = s * (1.f / 512.f);
    float q = 0; for (int i = 0; i < 8; ++i) { float d = f[i] - mean; q += d * d; }
    for (int o = 32; o > 0; o >>= 1) q += __shfl_xor(q, o);
    float var = q * (1.f / 512.f);
    float mb = rbf(mean), vb = rbf(var);
    float sd = rbf(sqrtf(rbf(vb + 1e-5f)));
    u32x4 o;
    for (int i = 0; i < 4; ++i) {
        unsigned short a = f2bf(rbf(f[2*i] - mb) / sd), b = f2bf(rbf(f[2*i+1] - mb) / sd);
        o[i] = (unsigned)a | ((unsigned)b << 16);
    }
    *(u32x4*)(y + (size_t)row * 512 + lane * 8) = o;
}

__device__ __forceinline__ unsigned off_b(unsigned row, unsigned ch) { return 256u * row + 16u * (ch ^ (((row & 3) << 2) | ((row >> 2) & 3))); }

template <int EPI, int AMODE>
__global__ __launch_bounds__(256) void gemm_kernel(const unsigned short* __restrict__ A, int lda, const unsigned short* __restrict__ A2,
                                                   const unsigned short* __restrict__ W, const unsigned short* __restrict__ W2, const unsigned short* __restrict__ W3,
                                                   int M, int N, int K, unsigned short* __restrict__ out,
                                                   const unsigned short* __restrict__ res, const unsigned short* __restrict__ bias) {
    __shared__ __attribute__((aligned(16))) char lds[32768];
    char* As = lds; char* Ws = lds + 16384;
    const int tid = threadIdx.x, wid = tid >> 6, lane = tid & 63, wr = wid >> 1, wc = wid & 1;
    int n0 = blockIdx.x * 128, m0 = blockIdx.y * 128;
    const unsigned short* Ap = A; const unsigned short* Wp = W; int nloc = n0;
    if (EPI == 0) {
        int which = n0 / 4096; nloc = n0 - which * 4096;
        if (which == 1) { Ap = A2; Wp = W2; } else if (which == 2) { Ap = A2; Wp = W3; }
    }
    const int ldw = (EPI == 0) ? 4096 : N;
    f32x16 acc[2][2] = {};
    for (int k0 = 0; k0 < K; k0 += 64) {
        __syncthreads();
        for (int i = 0; i < 4; ++i) {
            int c = tid + 256 * i; int row = c >> 3, ch = c & 7; int k = k0 + ch * 8;
            u32x4 v = {0, 0, 0, 0};
            if (k < K) {
                if (AMODE == 0) v = *(const u32x4*)(Ap + (size_t)(m0 + row) * lda + k);
                else v = *(const u32x4*)(Ap + ((size_t)(k >> 7) * 2048 + (m0 + row)) * 128 + (k & 127));
            }
            *(u32x4*)(As + row * 128 + ((ch ^ (row & 7)) << 4)) = v;
        }
        for (int i = 0; i < 4; ++i) {
            int c = tid + 256 * i; int row = c >> 4, ch = c & 15; int k = k0 + row, n = nloc + ch * 8;
            u32x4 v = {0, 0, 0, 0};
            if (k < K && n < ldw) v = *(const u32x4*)(Wp + (size_t)k * ldw + n);
            *(u32x4*)(Ws + off_b(row, ch)) = v;
        }
        __syncthreads();
        const unsigned wbase = (unsigned)(size_t)Ws;
        for (int ks = 0; ks < 4; ++ks) {
            bf16x8 a[2], b[2];
            for (int mi = 0; mi < 2; ++mi) { int row = wr * 64 + mi * 32 + (lane & 31), ch = 2 * ks + (lane >> 5);
                a[mi] = *(const bf16x8*)(As + row * 128 + ((ch ^ (row & 7)) << 4)); }
            for (int ni = 0; ni < 2; ++ni) {
                const unsigned h = lane >> 5, blk = (lane >> 4) & 1, q = (lane & 15) >> 2, p = lane & 3, c = wc * 2 + ni;
                s16x4 lo, hi;
                unsigned a0 = wbase + off_b(16 * ks + 8 * h + q, 4 * c + 2 * blk + (p >> 1)) + 8 * (p & 1);
                unsigned a1 = wbase + off_b(16 * ks + 8 * h + 4 + q, 4 * c + 2 * blk + (p >> 1)) + 8 * (p & 1);
                asm volatile("ds_read_b64_tr_b16 %0, %2\n\tds_read_b64_tr_b16 %1, %3\n\ts_waitcnt lgkmcnt(0)" : "=&v"(lo), "=&v"(hi) : "v"(a0), "v"(a1) : "memory");
                b[ni] = (bf16x8){lo[0], lo[1], lo[2], lo[3], hi[0], hi[1], hi[2], hi[3]};
            }
            for (int mi = 0; mi < 2; ++mi) for (int ni = 0; ni < 2; ++ni)
                acc[mi][ni] = __builtin_amdgcn_mfma_f32_32x32x16_bf16(a[mi], b[ni], acc[mi][ni], 0, 0, 0);
        }
    }
    for (int mi = 0; mi < 2; ++mi) for (int ni = 0; ni < 2; ++ni) for (int r = 0; r < 16; ++r) {
        int row = m0 + wr * 64 + mi * 32 + (r & 3) + 8 * (r >> 2) + 4 * (lane >> 5);
        int col = n0 + wc * 64 + ni * 32 + (lane & 31);
        if (col >= N) continue;
        float v = acc[mi][ni][r];
        if (EPI == 0) { int which = col >> 12, c = col & 4095; out[(size_t)which * 2048 * 4096 + ((size_t)(c >> 7) * 2048 + row) * 128 + (c & 127)] = f2bf(v); }
        else if (EPI == 1) { out[(size_t)row * 512 + col] = f2bf(bf2f(res[(size_t)row * 512 + col]) + rbf(v)); }
        else if (EPI == 2) { float hh = rbf(rbf(v) + bf2f(bias[col]));
            float inner = 0.7978845608028654f * (hh + 0.044715f * hh * hh * hh);
            float g = 0.5f * hh * (1.f + tanhf(inner));
            out[(size_t)row * N + col] = f2bf(g); }
        else { float t = rbf(rbf(v) + bf2f(bias[col])); out[(size_t)row * 512 + col] = f2bf(bf2f(res[(size_t)row * 512 + col]) + t); }
    }
}

#define RT(k,i)
#define RTC(k,i)
struct TArgs { const unsigned short* ctx; const unsigned short* x; const unsigned short* wo; const unsigned short* w1; const unsigned short* b1;
               const unsigned short* w2; const unsigned short* b2; unsigned short* h; unsigned short* g; unsigned short* out; const float* cs; float* stats; };
namespace tl { constexpr int NS = 10, BREG = 81920, LDSB = 163840; }
#define GLDS16(src, dst) __builtin_amdgcn_global_load_lds((const unsigned*)(src), (unsigned*)(dst), 16, 0, 0)
#define VMW(n) asm volatile("s_waitcnt vmcnt(%0)" :: "i"(n) : "memory")
#define BAR() __builtin_amdgcn_s_barrier()
template <int U, int N, int MUL, int ADD> struct TailW { static __device__ __forceinline__ void run() { VMW(MUL * U + ADD); BAR(); TailW<U - 1, N, MUL, ADD>::run(); } };
template <int N, int MUL, int ADD> struct TailW<-1, N, MUL, ADD> { static __device__ __forceinline__ void run() {} };

__device__ __forceinline__ float red8(float v) {
    v += __int_as_float(__builtin_amdgcn_update_dpp(0, __float_as_int(v), 0xB1, 0xf, 0xf, true));
    v += __int_as_float(__builtin_amdgcn_update_dpp(0, __float_as_int(v), 0x4E, 0xf, 0xf, true));
    v += __int_as_float(__builtin_amdgcn_update_dpp(0, __float_as_int(v), 0x141, 0xf, 0xf, true));
    return v;
}
template <int EPI>
__device__ __forceinline__ void g64_stage(const TArgs& g, char* lds, int rb, int ct, int wid, int lane, int tid) {
    constexpr int K = EPI == 1 ? 4096 : 1376, NT = (K + 63) / 64, NS = tl::NS;
    RT(EPI, 0);
    const int m0 = rb * 64, n0 = ct * 64, hi = lane >> 5, l31 = lane & 31;
    const unsigned short* Ap = EPI == 1 ? g.ctx : g.g; const unsigned short* Wp = EPI == 1 ? g.wo : g.w2;
    const int em = tid >> 3, ec = tid & 7;
    const u32x4 rres = *(const u32x4*)((EPI == 1 ? g.x : g.h) + (size_t)(m0 + em) * 512 + n0 + ec * 8);
    u32x4 rb2 = {0, 0, 0, 0}; if (EPI == 3) rb2 = *(const u32x4*)(g.b2 + n0 + ec * 8);
    if (wid >= 4) {
        const int lw = wid - 4;
        unsigned aoff[2], boff[2];
#pragma unroll
        for (int i = 0; i < 2; ++i) { const int pos = (lw * 2 + i) * 64 + lane, row = pos >> 3;
            aoff[i] = (unsigned)(m0 + row) * (EPI == 1 ? 128 : GS) + (((pos & 7) ^ ((row >> 1) & 7)) << 3);
            boff[i] = (unsigned)row * 512 + n0 + (((pos & 7) ^ ((row & 3) << 1)) << 3); }
        char* la = lds + lw * 2048 + lane * 16;
#define T_ISS(t, sl) do { int kt = (t) * 64; if (EPI == 3 && kt + 64 > K) kt = K - 64; \
        const unsigned short* ab = EPI == 1 ? Ap + ((size_t)(kt >> 7) * 2048 * 128 + (kt & 127)) : Ap + kt; const unsigned short* wb = Wp + (size_t)kt * 512; \
        GLDS16(ab + aoff[0], la + (sl) * 8192); GLDS16(ab + aoff[1], la + (sl) * 8192 + 1024); \
        GLDS16(wb + boff[0], la + tl::BREG + (sl) * 8192); GLDS16(wb + boff[1], la + tl::BREG + (sl) * 8192 + 1024); } while (0)
#pragma unroll
        for (int p = 0; p < NS - 2; ++p) T_ISS(p, p);
        int is = NS - 2;
        for (int t = 0; t + NS - 2 < NT; ++t) {
            VMW(4 * (NS - 3)); BAR();
            T_ISS(t + NS - 2, is);
            is = is + 1 == NS ? 0 : is + 1;
        }
        TailW<NS - 3, NS, 4, 0>::run();
#undef T_ISS
        BAR();
    } else {
        f32x16 acc[2][2];
#pragma unroll
        for (int i = 0; i < 2; ++i)
#pragma unroll
            for (int j = 0; j < 2; ++j) acc[i][j] = f32x16{};
        const unsigned lbase = (unsigned)(size_t)lds;
        const unsigned blk = (lane >> 4) & 1, q4 = (lane & 15) >> 2, p4 = lane & 3;
        const unsigned ard = lbase + l31 * 128 + (((2 * wid + hi) ^ ((l31 >> 1) & 7)) << 4);
        const unsigned brd = lbase + tl::BREG + (16 * wid + 8 * hi + q4) * 128 + (((2 * blk + (p4 >> 1)) ^ (q4 << 1)) << 4) + 8 * (p4 & 1);
        unsigned so = 0;
        bf16x8 a0[2], a1[2]; s16x4 b00[2], b01[2], b10[2], b11[2];
#define T_RD(sl) do { BAR(); asm volatile("ds_read_b128 %0, %6\n\tds_read_b64_tr_b16 %2, %7\n\tds_read_b64_tr_b16 %3, %7 offset:512\n\tds_read_b128 %1, %6 offset:4096\n\t" \
                         "ds_read_b64_tr_b16 %4, %8\n\tds_read_b64_tr_b16 %5, %8 offset:512" \
                         : "=&v"(a0[sl]), "=&v"(a1[sl]), "=&v"(b00[sl]), "=&v"(b01[sl]), "=&v"(b10[sl]), "=&v"(b11[sl]) : "v"(ard + so), "v"(brd + so), "v"((brd + so) ^ 64u) : "memory"); \
            so = so + 8192 == NS * 8192 ? 0 : so + 8192; } while (0)
#define T_MM(sl, n) do { asm volatile("s_waitcnt lgkmcnt(%0)" :: "i"(n) : "memory"); SBAR(); \
            const bf16x8 b0 = {b00[sl][0], b00[sl][1], b00[sl][2], b00[sl][3], b01[sl][0], b01[sl][1], b01[sl][2], b01[sl][3]}, b1 = {b10[sl][0], b10[sl][1], b10[sl][2], b10[sl][3], b11[sl][0], b11[sl][1], b11[sl][2], b11[sl][3]}; \
            acc[0][0] = __builtin_amdgcn_mfma_f32_32x32x16_bf16(b0, a0[sl], acc[0][0], 0, 0, 0); \
            acc[1][0] = __builtin_amdgcn_mfma_f32_32x32x16_bf16(b0, a1[sl], acc[1][0], 0, 0, 0); \
            acc[0][1] = __builtin_amdgcn_mfma_f32_32x32x16_bf16(b1, a0[sl], acc[0][1], 0, 0, 0); \
            acc[1][1] = __builtin_amdgcn_mfma_f32_32x32x16_bf16(b1, a1[sl], acc[1][1], 0, 0, 0); SBAR(); } while (0)
        T_RD(0);
        RT(EPI, 1); RTC(EPI, 6);
        for (int t = 1; t + 1 < NT; t += 2) { T_RD(1); T_MM(0, 6); T_RD(0); T_MM(1, 6); }
        T_RD(1); T_MM(0, 6);
        if (!(EPI == 3 && wid < 2)) T_MM(1, 0); else asm volatile("s_waitcnt lgkmcnt(0)" ::: "memory");
#undef T_RD
#undef T_MM
        RT(EPI, 2); RTC(EPI, 7);
        BAR();
#pragma unroll
        for (int i = 0; i < 2; ++i)
#pragma unroll
            for (int j = 0; j < 2; ++j)
#pragma unroll
                for (int q = 0; q < 4; ++q) { const int ml = i * 32 + l31, cn = 8 * j + 2 * q + hi;
                    const f32x4 w = {acc[i][j][4 * q], acc[i][j][4 * q + 1], acc[i][j][4 * q + 2], acc[i][j][4 * q + 3]};
                    *(f32x4*)(lds + wid * 16384 + ml * 256 + ((cn ^ (ml & 15)) << 4)) = w; }
    }
    __syncthreads();
    RT(EPI, 3);
    {
        f32x4 s0[4], s1[4];
#pragma unroll
        for (int w = 0; w < 4; ++w) { s0[w] = *(const f32x4*)(lds + w * 16384 + em * 256 + (((2 * ec) ^ (em & 15)) << 4));
                                      s1[w] = *(const f32x4*)(lds + w * 16384 + em * 256 + (((2 * ec + 1) ^ (em & 15)) << 4)); }
        const f32x4 t0 = (s0[0] + s0[1]) + (s0[2] + s0[3]), t1 = (s1[0] + s1[1]) + (s1[2] + s1[3]);
        const float f[8] = {t0[0], t0[1], t0[2], t0[3], t1[0], t1[1], t1[2], t1[3]};
        u32x4 o;
#pragma unroll
        for (int i = 0; i < 4; ++i) {
            unsigned r = cvtpk(f[2 * i], f[2 * i + 1]);
            if (EPI == 3) r = cvtpk(__uint_as_float(r << 16) + __uint_as_float(rb2[i] << 16), __uint_as_float(r & 0xffff0000u) + __uint_as_float(rb2[i] & 0xffff0000u));
            o[i] = cvtpk(__uint_as_float(r << 16) + __uint_as_float(rres[i] << 16), __uint_as_float(r & 0xffff0000u) + __uint_as_float(rres[i] & 0xffff0000u)); }
        *(u32x4*)((EPI == 1 ? g.h : g.out) + (size_t)(m0 + em) * 512 + n0 + ec * 8) = o;
        if (EPI == 1) {
            float hv[8];
#pragma unroll
            for (int i = 0; i < 4; ++i) { hv[2 * i] = __uint_as_float(o[i] << 16); hv[2 * i + 1] = __uint_as_float(o[i] & 0xffff0000u); }
            float sm = ((hv[0] + hv[1]) + (hv[2] + hv[3])) + ((hv[4] + hv[5]) + (hv[6] + hv[7]));
            sm = red8(sm);
            const float mt = sm * (1.f / 64.f); float q = 0;
#pragma unroll
            for (int i = 0; i < 8; ++i) { const float d = hv[i] - mt; q = fmaf(d, d, q); }
            q = red8(q);
            if (ec == 0) *(float2*)(g.stats + ((size_t)(m0 + em) * 8 + ct) * 2) = make_float2(sm, q);
        }
    }
}

__device__ __forceinline__ void ffn1_stage(const TArgs& g, char* lds, int rb, int ct, int wid, int lane, int tid) {
    RT(2, 0);
    const int m0 = rb * 64, n0 = 32 * ((43 * ct) >> 3), hi = lane >> 5, l31 = lane & 31;
    if (wid >= 4) {
        const int lw = wid - 4;
        unsigned aoff[2], boff[6]; char* la = lds + lw * 2048 + lane * 16; char* lb = lds + 8192 + lw * 6144 + lane * 16;
#pragma unroll
        for (int i = 0; i < 2; ++i) { const int pos = (lw * 2 + i) * 64 + lane, row = pos >> 3;
            aoff[i] = (unsigned)(m0 + row) * 512 + (((pos & 7) ^ ((row >> 1) & 7)) << 3); }
#pragma unroll
        for (int i = 0; i < 6; ++i) { const int pos = (lw * 6 + i) * 64 + lane, row = (pos >> 3) & 63;
            const int c = (pos >> 9) * 8 + ((pos & 7) ^ ((row & 3) << 1));
            boff[i] = (unsigned)row * 1376 + n0 + ((ALN && ((row + (n0 >> 5)) & 1)) ? (c + 20) % 24 : c) * 8; }
#define F_ISS(t, sl) do { const unsigned short* wb = g.w1 + (size_t)(t) * 64 * 1376; const unsigned short* ab = g.h + (t) * 64; \
        _Pragma("unroll") for (int i = 0; i < 2; ++i) GLDS16(ab + aoff[i], la + (sl) * 32768 + i * 1024); \
        _Pragma("unroll") for (int i = 0; i < 6; ++i) GLDS16(wb + boff[i], lb + (sl) * 32768 + i * 1024); } while (0)
#pragma unroll
        for (int p = 0; p < F1P; ++p) F_ISS(p, p);
        VMW(8 * (F1P - 1)); BAR();
#pragma unroll
        for (int p = F1P; p < 5; ++p) F_ISS(p, p);
        VMW(24); BAR(); F_ISS(5, 0);
        VMW(24); BAR(); F_ISS(6, 1);
        VMW(24); BAR(); F_ISS(7, 2);
        VMW(24); BAR();
        VMW(16); BAR();
        VMW(8); BAR();
        VMW(0); BAR();
        BAR();
#undef F_ISS
    } else {
        const int mb = wid & 1, nb0 = 3 * (wid >> 1);
        f32x16 acc[3]; f32x4 acs[3][4];
#pragma unroll
        for (int j = 0; j < 3; ++j) { acc[j] = f32x16{};
#pragma unroll
            for (int q = 0; q < 4; ++q) acs[j][q] = *(const f32x4*)(g.cs + n0 + (nb0 + j) * 32 + 8 * q + 4 * hi); }
        uint2 bpre[3][4];
#pragma unroll
        for (int j = 0; j < 3; ++j)
#pragma unroll
            for (int q = 0; q < 4; ++q) bpre[j][q] = *(const uint2*)(g.b1 + n0 + (nb0 + j) * 32 + 8 * q + 4 * hi);
        f32x4 st[4];
#pragma unroll
        for (int i = 0; i < 4; ++i) st[i] = *(const f32x4*)(g.stats + (size_t)(m0 + mb * 32 + l31) * 16 + i * 4);
        const unsigned lbase = (unsigned)(size_t)lds;
        const unsigned blk = (lane >> 4) & 1, q4 = (lane & 15) >> 2, p4 = lane & 3;
        const unsigned ard = lbase + (mb * 32 + l31) * 128 + ((hi ^ ((l31 >> 1) & 7)) << 4);
        unsigned brd[3];
#pragma unroll
        for (int j = 0; j < 3; ++j) { const int nb = (nb0 + j + (ALN ? (q4 + (n0 >> 5)) & 1 : 0)) % 6;
            brd[j] = lbase + 8192 + (nb >> 1) * 8192 + (8 * hi + q4) * 128 + (((4 * (nb & 1) + 2 * blk + (p4 >> 1)) ^ (q4 << 1)) << 4) + 8 * (p4 & 1); }
        bf16x8 a[2]; s16x4 bl[2][3], bh[2][3];
#define F_RD(ks, sl, bo) do { asm volatile("ds_read_b128 %0, %1" : "=&v"(a[sl]) : "v"((ard + (bo)) ^ ((ks) << 5)) : "memory"); \
        _Pragma("unroll") for (int j = 0; j < 3; ++j) { asm volatile("ds_read_b64_tr_b16 %0, %1 offset:%2" : "=&v"(bl[sl][j]) : "v"(brd[j] + (bo)), "i"((ks) * 2048) : "memory"); \
            asm volatile("ds_read_b64_tr_b16 %0, %1 offset:%2" : "=&v"(bh[sl][j]) : "v"(brd[j] + (bo)), "i"((ks) * 2048 + 512) : "memory"); } } while (0)
#define F_MM(sl) do { _Pragma("unroll") for (int j = 0; j < 3; ++j) { \
        const bf16x8 bf_ = {bl[sl][j][0], bl[sl][j][1], bl[sl][j][2], bl[sl][j][3], bh[sl][j][0], bh[sl][j][1], bh[sl][j][2], bh[sl][j][3]}; \
        acc[j] = __builtin_amdgcn_mfma_f32_32x32x16_bf16(bf_, a[sl], acc[j], 0, 0, 0); } } while (0)
        const float mean = (((st[0][0] + st[0][2]) + (st[1][0] + st[1][2])) + ((st[2][0] + st[2][2]) + (st[3][0] + st[3][2]))) * (1.f / 512.f);
        float m2 = 0;
#pragma unroll
        for (int i = 0; i < 4; ++i) { const float d0 = st[i][0] * (1.f / 64.f) - mean, d1 = st[i][2] * (1.f / 64.f) - mean; m2 += (st[i][1] + st[i][3]) + 64.f * (d0 * d0 + d1 * d1); }
        float mbn = -rbf(mean), rsd = 1.f / rbf(sqrtf(rbf(rbf(m2 * (1.f / 512.f)) + 1e-5f)));
        mbn *= rsd;
#pragma unroll
        for (int j = 0; j < 3; ++j)
#pragma unroll
            for (int q = 0; q < 4; ++q) { const uint2 bb = bpre[j][q];
                acs[j][q] = (f32x4){fmaf(acs[j][q][0], mbn, __uint_as_float(bb.x << 16)), fmaf(acs[j][q][1], mbn, __uint_as_float(bb.x & 0xffff0000u)),
                                    fmaf(acs[j][q][2], mbn, __uint_as_float(bb.y << 16)), fmaf(acs[j][q][3], mbn, __uint_as_float(bb.y & 0xffff0000u))};
                asm volatile("" : "+v"(acs[j][q])); }
        asm volatile("" : "+v"(rsd));
        unsigned bo = 0;
#pragma unroll 1
        for (int t = 0; t < 8; ++t) {
            BAR();
            if (t == 0) { RT(2, 1); RTC(2, 6); }
            F_RD(0, 0, bo);
            F_RD(1, 1, bo); asm volatile("s_waitcnt lgkmcnt(7)" ::: "memory"); SBAR(); F_MM(0); SBAR();
            F_RD(2, 0, bo); asm volatile("s_waitcnt lgkmcnt(7)" ::: "memory"); SBAR(); F_MM(1); SBAR();
            F_RD(3, 1, bo); asm volatile("s_waitcnt lgkmcnt(7)" ::: "memory"); SBAR(); F_MM(0); SBAR();
            asm volatile("s_waitcnt lgkmcnt(0)" ::: "memory"); SBAR(); F_MM(1); SBAR();
            bo = bo == 4 * 32768 ? 0 : bo + 32768;
        }
#undef F_RD
#undef F_MM
        RT(2, 2); RTC(2, 7);
        BAR();
#pragma unroll
        for (int j = 0; j < 3; ++j)
#pragma unroll
            for (int q = 0; q < 4; ++q) {
                const int ml = mb * 32 + l31, nl = (nb0 + j) * 32 + 8 * q + 4 * hi;
                float hh[4];
#pragma unroll
                for (int e = 0; e < 4; ++e) hh[e] = fmaf(acc[j][4 * q + e], rsd, acs[j][q][e]);
                float gv[4];
#pragma unroll
                for (int e = 0; e < 4; ++e) {
                    constexpr float C1 = 0.7978845608028654f * 2.f * 1.4426950408889634f, C3 = C1 * 0.044715f;
                    const float ex = __builtin_amdgcn_exp2f(hh[e] * fmaf(hh[e] * hh[e], C3, C1));
                    gv[e] = fmaf(-hh[e], __builtin_amdgcn_rcpf(ex + 1.f), hh[e]); }
                uint2 w; w.x = cvtpk(gv[0], gv[1]); w.y = cvtpk(gv[2], gv[3]);
                *(uint2*)(lds + ml * 400 + nl * 2) = w; }
    }
    __syncthreads();
    RT(2, 3);
    const int nc = 4 * (((43 * ct + 43) >> 3) - ((43 * ct) >> 3));
#pragma unroll
    for (int i = 0; i < 3; ++i) { const int c = tid + i * 512, ml = c / 24, cc = c % 24;
        if (cc < nc) *(u32x4*)(g.g + (size_t)(m0 + ml) * GS + n0 + cc * 8) = *(const u32x4*)(lds + ml * 400 + cc * 16); }
}

template <int ST>
__global__ __launch_bounds__(512) void tail_kernel(TArgs g) {
    extern __shared__ __attribute__((aligned(16))) char lds[];
    const int tid = threadIdx.x, wid = __builtin_amdgcn_readfirstlane(tid >> 6), lane = tid & 63;
    const int id = blockIdx.x, rb = 4 * (id & 7) + ((id >> 3) & 3), ct = id >> 5;
    if (ST == 1) g64_stage<1>(g, lds, rb, ct, wid, lane, tid);
    if (ST == 2) ffn1_stage(g, lds, rb, ct, wid, lane, tid);
    if (ST == 3) g64_stage<3>(g, lds, rb, ct, wid, lane, tid);
}
extern "C" void kernel_launch(void* const* d_in, const int* in_sizes, int n_in, void* d_out, int out_size, void* d_ws,
                              size_t ws_size, hipStream_t stream) {
    typedef unsigned short u16;
    const u16* x = (const u16*)d_in[0]; const u16* ckv = (const u16*)d_in[1]; const u16* wq = (const u16*)d_in[2];
    const u16* wuk = (const u16*)d_in[3]; const u16* wuv = (const u16*)d_in[4]; const u16* wo = (const u16*)d_in[5];
    const u16* w1 = (const u16*)d_in[7]; const u16* b1 = (const u16*)d_in[8]; const u16* w2 = (const u16*)d_in[9]; const u16* b2 = (const u16*)d_in[10];
    u16* out = (u16*)d_out;
    u16* ws = (u16*)d_ws;
    u16* xn = ws;
    u16* qkv = xn + 2048 * 512;
    u16* ctx = qkv + 3 * 2048 * 4096;
    u16* h = ctx + 2048 * 4096;
    u16* gg = h + 2048 * 512;
    unsigned* cnt = (unsigned*)(gg + 2048 * 1408 + 1024);
    (void)hipFuncSetAttribute((const void*)tail_kernel<1>, hipFuncAttributeMaxDynamicSharedMemorySize, tl::LDSB);
    (void)hipFuncSetAttribute((const void*)tail_kernel<2>, hipFuncAttributeMaxDynamicSharedMemorySize, tl::LDSB);
    (void)hipFuncSetAttribute((const void*)tail_kernel<3>, hipFuncAttributeMaxDynamicSharedMemorySize, tl::LDSB);
    unsigned* kcnt = cnt + 8192 + 2048 * 16;
    float* cs = (float*)(kcnt + 4096); u16* ckp = (u16*)(cs + 4096);
    pre_kernel<<<427, 512, 0, stream>>>(x, xn, kcnt, w1, cs, ckv, ckp);
    const bf16* q = (const bf16*)qkv; const bf16* k = q + 2048 * 4096; const bf16* v = k + 2048 * 4096;
    (void)hipFuncSetAttribute((const void*)attn2_kernel, hipFuncAttributeMaxDynamicSharedMemorySize, at2::LDSB);
    attn2_kernel<<<256, 512, at2::LDSB, stream>>>(xn, wq, (bf16*)qkv, (bf16*)k, (bf16*)v, (bf16*)ctx, wo, w1, w2, ckp, wuk, wuv, kcnt);
    TArgs t{ctx, x, wo, w1, b1, w2, b2, h, gg, out, cs, (float*)(cnt + 4096)};
    tail_kernel<1><<<256, 512, tl::LDSB, stream>>>(t);
    tail_kernel<2><<<256, 512, tl::LDSB, stream>>>(t);
    tail_kernel<3><<<256, 512, tl::LDSB, stream>>>(t);
}
```
